```python
import torch

def _softmax(x, axis=-1):
    return torch.softmax(x, dim=axis)
def _gelu(x):
    return torch.nn.functional.gelu(x, approximate="tanh")
def _silu(x):
    return torch.nn.functional.silu(x)
def _sigmoid(x):
    return torch.sigmoid(x)
def _layernorm(x, axis=-1, eps=1e-5):
    m = x.mean(dim=axis, keepdim=True)
    v = x.var(dim=axis, keepdim=True, unbiased=False)
    return (x - m) / torch.sqrt(v + eps)
def _scatter_add(tgt, idx, src, axis=0):
    return tgt.clone().index_add_(axis, idx, src)
def _segment_sum(src, seg, n):
    out = torch.zeros((n,) + src.shape[1:], dtype=src.dtype)
    return out.index_add_(0, seg, src)
def _pad(x, pw):
    flat = tuple(v for pair in reversed(pw) for v in pair)
    return torch.nn.functional.pad(x, flat)

kernel_name = 'standalone__qk_norm__prompt'

def setup_inputs():

    _w = (lambda a: (lambda t: t.to(torch.bfloat16) if t.is_floating_point() else t)(torch.as_tensor(a)))
    B, H, S, D = 2, 32, 2048, 576
    Dh = D // H
    return {
        "q": _w((lambda *s: torch.randn(*s, dtype=torch.bfloat16) * 0.05)(B, H, S, Dh)),
        "k": _w((lambda *s: torch.randn(*s, dtype=torch.bfloat16) * 0.05)(B, H, S, Dh)),
        "v": _w((lambda *s: torch.randn(*s, dtype=torch.bfloat16) * 0.4)(B, H, S, Dh)),
        "t": _w(torch.abs((lambda *s: torch.randn(*s, dtype=torch.bfloat16) * 0.4)(H)) + 1.0),
    }

def reference(q, k, v, t):
    eps = 1e-6
    qn = q / (torch.sqrt((q * q).sum(axis=-1, keepdims=True)) + eps)
    kn = k / (torch.sqrt((k * k).sum(axis=-1, keepdims=True)) + eps)
    logits = torch.einsum('bhqd,bhkd->bhqk', qn, kn) * t[None, :, None, None]
    attn = _softmax(logits, axis=-1)
    return torch.einsum('bhqk,bhkd->bhqd', attn, v)
```

```cpp
#include <hip/hip_runtime.h>
#include <hip/hip_bf16.h>
#include <stdint.h>

typedef float f32x16 __attribute__((ext_vector_type(16)));
typedef float f32x8 __attribute__((ext_vector_type(8)));
typedef __bf16 bf16x8 __attribute__((ext_vector_type(8)));
typedef __bf16 bf16x16 __attribute__((ext_vector_type(16)));
typedef __bf16 bf16x2 __attribute__((ext_vector_type(2)));
typedef short s16x4 __attribute__((ext_vector_type(4)));
typedef unsigned u32x4 __attribute__((ext_vector_type(4)));
typedef unsigned u32x2 __attribute__((ext_vector_type(2)));
typedef u32x4 u32x4_a4 __attribute__((aligned(4)));
typedef u32x2 u32x2_a4 __attribute__((aligned(4)));

constexpr int SEQ = 2048, DH = 18, NHEAD = 32, NBH = 64;
constexpr int NTILE = SEQ / 32;
constexpr int LDS_KM = 0;
constexpr int LDS_KE = LDS_KM + SEQ * 32;
constexpr int LDS_VT = LDS_KE + SEQ * 4;
constexpr int VBLK = 19 * 16;
constexpr int LDS_END = LDS_VT + (SEQ / 8) * VBLK;
constexpr int LDS_BYTES = LDS_END + 256;
constexpr float LOG2E = 1.4426950408889634f;
constexpr float EPS = 1e-6f;
constexpr float FAST_LIMIT = 16.0f;

typedef __attribute__((address_space(3))) char* lds_ptr;
typedef __attribute__((address_space(3))) const char* lds_cptr;

__device__ __forceinline__ unsigned cvtpk(float lo, float hi) {
    bf16x2 r; r[0] = (__bf16)lo; r[1] = (__bf16)hi; return __builtin_bit_cast(unsigned, r);
}
__device__ __forceinline__ float bflo(unsigned x) { return __uint_as_float(x << 16); }
__device__ __forceinline__ float bfhi(unsigned x) { return __uint_as_float(x & 0xffff0000u); }
__device__ __forceinline__ float sumsq9(const unsigned* w) {
    float a = 0.f, b = 0.f;
#pragma unroll
    for (int i = 0; i < 9; ++i) {
        bf16x2 v = __builtin_bit_cast(bf16x2, w[i]);
        if (i & 1) b = __builtin_amdgcn_fdot2_f32_bf16(v, v, b, false);
        else a = __builtin_amdgcn_fdot2_f32_bf16(v, v, a, false);
    }
    return a + b;
}
__device__ __forceinline__ unsigned scale2(unsigned w, float r) { return cvtpk(bflo(w) * r, bfhi(w) * r); }

#define MFMA16(a, b, c) __builtin_amdgcn_mfma_f32_32x32x16_bf16(__builtin_bit_cast(bf16x8, a), __builtin_bit_cast(bf16x8, b), c, 0, 0, 0)
#define MFMA8(a, b, c) __builtin_amdgcn_mfma_f32_32x32x8bf16_1k(__builtin_bit_cast(s16x4, a), __builtin_bit_cast(s16x4, b), c, 0, 0, 0)
#define SMF(a, b, c) __builtin_amdgcn_smfmac_f32_32x32x32_bf16(__builtin_bit_cast(bf16x8, a), __builtin_bit_cast(bf16x16, b), c, (int)0xDDDDDDDD, 0, 0)
#define LO8(x) __builtin_shufflevector(x, x, 0, 1, 2, 3, 4, 5, 6, 7)
#define HI8(x) __builtin_shufflevector(x, x, 8, 9, 10, 11, 12, 13, 14, 15)

__global__ __launch_bounds__(1024) void qknorm_attn(const uint16_t* __restrict__ Q, const uint16_t* __restrict__ K,
                                                    const uint16_t* __restrict__ V, const uint16_t* __restrict__ T,
                                                    uint16_t* __restrict__ O) {
    extern __shared__ __attribute__((aligned(16))) char smem[];
    const lds_ptr lds = (lds_ptr)smem;
    const int tid = threadIdx.x, lane = tid & 63, r32 = lane & 31, hi = lane >> 5;
    const int wid = __builtin_amdgcn_readfirstlane(tid >> 6);
    const int bid = blockIdx.x;
    const int bh = (bid & 7) + 8 * (bid >> 5), qc = (bid >> 3) & 3;
    const size_t hb = (size_t)bh * SEQ * DH;

    unsigned kw[2][9], vw[18], qw[9]; u32x4 qsel;
    {
        const char* kp = (const char*)(K + hb) + tid * 36;
#pragma unroll
        for (int i = 0; i < 2; ++i) {
            u32x4 a = *(const u32x4_a4*)(kp + i * 1024 * 36), b = *(const u32x4_a4*)(kp + i * 1024 * 36 + 16);
            unsigned c = *(const unsigned*)(kp + i * 1024 * 36 + 32);
            kw[i][0] = a[0]; kw[i][1] = a[1]; kw[i][2] = a[2]; kw[i][3] = a[3];
            kw[i][4] = b[0]; kw[i][5] = b[1]; kw[i][6] = b[2]; kw[i][7] = b[3]; kw[i][8] = c;
        }
        const char* vp = (const char*)(V + hb) + tid * 72;
#pragma unroll
        for (int i = 0; i < 4; ++i) {
            u32x4 a = *(const u32x4_a4*)(vp + i * 16);
            vw[4 * i] = a[0]; vw[4 * i + 1] = a[1]; vw[4 * i + 2] = a[2]; vw[4 * i + 3] = a[3];
        }
        u32x2 e = *(const u32x2_a4*)(vp + 64); vw[16] = e[0]; vw[17] = e[1];
        const char* qp = (const char*)(Q + hb) + (qc * 512 + wid * 32 + r32) * 36;
        u32x4 a = *(const u32x4_a4*)(qp), b = *(const u32x4_a4*)(qp + 16);
        unsigned c = *(const unsigned*)(qp + 32);
        qw[0] = a[0]; qw[1] = a[1]; qw[2] = a[2]; qw[3] = a[3]; qw[4] = b[0]; qw[5] = b[1]; qw[6] = b[2]; qw[7] = b[3]; qw[8] = c;
        qsel = hi ? b : a;
    }
    const float tval = __uint_as_float((unsigned)T[bh & (NHEAD - 1)] << 16);
    const float cs = tval * LOG2E;
    const bool fast = __builtin_fabsf(cs) <= FAST_LIMIT;

#pragma unroll
    for (int i = 0; i < 2; ++i) {
        const int key = tid + i * 1024;
        const float r = __builtin_amdgcn_rcpf(__builtin_amdgcn_sqrtf(sumsq9(kw[i])) + EPS);
        u32x4 a, b;
#pragma unroll
        for (int j = 0; j < 4; ++j) { a[j] = scale2(kw[i][j], r); b[j] = scale2(kw[i][4 + j], r); }
        const unsigned e = scale2(kw[i][8], r);
        lds_ptr p = lds + LDS_KM + (key >> 5) * 1024 + (key & 31) * 16;
        *(__attribute__((address_space(3))) u32x4*)(p) = a;
        *(__attribute__((address_space(3))) u32x4*)(p + 512) = b;
        *(__attribute__((address_space(3))) unsigned*)(lds + LDS_KE + key * 4) = e;
    }
    {
        lds_ptr p = lds + LDS_VT + (tid >> 2) * VBLK + (tid & 3) * 4;
#pragma unroll
        for (int j = 0; j < 9; ++j) {
            const unsigned x = vw[j], y = vw[9 + j];
            *(__attribute__((address_space(3))) unsigned*)(p + (2 * j) * 16) = __builtin_amdgcn_perm(y, x, 0x05040100u);
            *(__attribute__((address_space(3))) unsigned*)(p + (2 * j + 1) * 16) = __builtin_amdgcn_perm(y, x, 0x07060302u);
        }
        *(__attribute__((address_space(3))) unsigned*)(p + 18 * 16) = 0x3F803F80u;
    }
    u32x4 qm; u32x2 qe;
    {
        const float r = cs * __builtin_amdgcn_rcpf(__builtin_amdgcn_sqrtf(sumsq9(qw)) + EPS);
#pragma unroll
        for (int j = 0; j < 4; ++j) qm[j] = scale2(qsel[j], r);
        const float shift = fast ? -(__builtin_fabsf(cs) * 1.015625f) : 0.f;
        qe[0] = hi ? 0u : scale2(qw[8], r);
        qe[1] = hi ? 0u : cvtpk(shift, 0.f);
    }
    __syncthreads();

    const lds_cptr kmp = (lds_cptr)lds + LDS_KM + lane * 16;
    const lds_cptr kep = (lds_cptr)lds + LDS_KE + r32 * 4;
    const lds_cptr vtp = (lds_cptr)lds + LDS_VT + (hi * 19 + r32) * 16;
#define LDKM(t) (*(const __attribute__((address_space(3))) u32x4*)(kmp + (t) * 1024))
#define LDKE(t) (*(const __attribute__((address_space(3))) unsigned*)(kep + (t) * 128))
#define LDVT(t, s) (*(const __attribute__((address_space(3))) u32x4*)(vtp + ((t) * 4 + (s) * 2) * VBLK))
    f32x16 o = {};
    const f32x16 zero = {};
    if (fast) {
        f32x16 sA, sB;
        { u32x2 ke; ke[0] = LDKE(0); ke[1] = 0x00003F80u; sA = MFMA8(ke, qe, zero); sA = MFMA16(LDKM(0), qm, sA); }
        for (int t = 0; t < NTILE; t += 2) {
            { u32x2 ke; ke[0] = LDKE(t + 1); ke[1] = 0x00003F80u; sB = MFMA8(ke, qe, zero); sB = MFMA16(LDKM(t + 1), qm, sB); }
#pragma unroll
            for (int r = 0; r < 16; ++r) sA[r] = __builtin_amdgcn_exp2f(sA[r]);
            o = SMF(LDVT(t, 0), LO8(sA), o);
            o = SMF(LDVT(t, 1), HI8(sA), o);
            const int tn = (t + 2 < NTILE) ? t + 2 : t;
            { u32x2 ke; ke[0] = LDKE(tn); ke[1] = 0x00003F80u; sA = MFMA8(ke, qe, zero); sA = MFMA16(LDKM(tn), qm, sA); }
#pragma unroll
            for (int r = 0; r < 16; ++r) sB[r] = __builtin_amdgcn_exp2f(sB[r]);
            o = SMF(LDVT(t + 1, 0), LO8(sB), o);
            o = SMF(LDVT(t + 1, 1), HI8(sB), o);
        }
    } else {
        float m = -INFINITY;
        for (int t = 0; t < NTILE; ++t) {
            f32x16 s;
            { u32x2 ke; ke[0] = LDKE(t); ke[1] = 0x00003F80u; s = MFMA8(ke, qe, zero); s = MFMA16(LDKM(t), qm, s); }
            float mx = s[0];
#pragma unroll
            for (int r = 1; r < 16; ++r) mx = __builtin_fmaxf(mx, s[r]);
            mx = __builtin_fmaxf(mx, __shfl_xor(mx, 32));
            const float mn = __builtin_fmaxf(m, mx);
            const float alpha = __builtin_amdgcn_exp2f(m - mn);
            m = mn;
#pragma unroll
            for (int r = 0; r < 16; ++r) { o[r] *= alpha; s[r] = __builtin_amdgcn_exp2f(s[r] - mn); }
            o = SMF(LDVT(t, 0), LO8(s), o);
            o = SMF(LDVT(t, 1), HI8(s), o);
        }
    }
    {
        const float l = __shfl(o[10], r32);
        const float inv = 1.0f / l;
        char* op = (char*)(O + hb) + (qc * 512 + wid * 32 + r32) * 36 + hi * 8;
        u32x2 a, b;
        a[0] = cvtpk(o[0] * inv, o[1] * inv); a[1] = cvtpk(o[2] * inv, o[3] * inv);
        b[0] = cvtpk(o[4] * inv, o[5] * inv); b[1] = cvtpk(o[6] * inv, o[7] * inv);
        *(u32x2_a4*)(op) = a;
        *(u32x2_a4*)(op + 16) = b;
        if (!hi) *(unsigned*)(op + 32) = cvtpk(o[8] * inv, o[9] * inv);
    }
}

extern "C" void kernel_launch(void* const* d_in, const int* in_sizes, int n_in,
                              void* d_out, int out_size, void* d_ws, size_t ws_size,
                              hipStream_t stream) {
    (void)in_sizes; (void)n_in; (void)out_size; (void)d_ws; (void)ws_size;
    (void)hipFuncSetAttribute((const void*)qknorm_attn, hipFuncAttributeMaxDynamicSharedMemorySize, LDS_BYTES);
    qknorm_attn<<<NBH * 4, 1024, LDS_BYTES, stream>>>((const uint16_t*)d_in[0], (const uint16_t*)d_in[1],
                                                      (const uint16_t*)d_in[2], (const uint16_t*)d_in[3],
                                                      (uint16_t*)d_out);
}
```

```cpp
#include <hip/hip_runtime.h>
#include <hip/hip_bf16.h>
#include <stdint.h>

typedef float f32x16 __attribute__((ext_vector_type(16)));
typedef float f32x8 __attribute__((ext_vector_type(8)));
typedef __bf16 bf16x8 __attribute__((ext_vector_type(8)));
typedef __bf16 bf16x16 __attribute__((ext_vector_type(16)));
typedef __bf16 bf16x2 __attribute__((ext_vector_type(2)));
typedef short s16x4 __attribute__((ext_vector_type(4)));
typedef unsigned u32x4 __attribute__((ext_vector_type(4)));
typedef unsigned u32x2 __attribute__((ext_vector_type(2)));
typedef u32x4 u32x4_a4 __attribute__((aligned(4)));
typedef u32x2 u32x2_a4 __attribute__((aligned(4)));

constexpr int SEQ = 2048, DH = 18, NHEAD = 32, NBH = 64;
constexpr int NTILE = SEQ / 32;
constexpr int VBLK = 19 * 16;
constexpr int LDS_KE = 256;
constexpr int LDS_KM = LDS_KE + SEQ * 4;
constexpr int LDS_VT = LDS_KM + SEQ * 32;
constexpr int LDS_END = LDS_VT + (SEQ / 8) * VBLK;
constexpr int LDS_BYTES = LDS_END + 2048;
constexpr float LOG2E = 1.4426950408889634f;
constexpr float EPS = 1e-6f;
constexpr float FAST_LIMIT = 16.0f;

typedef __attribute__((address_space(3))) char* lds_ptr;
typedef __attribute__((address_space(3))) const char* lds_cptr;

__device__ __forceinline__ unsigned cvtpk(float lo, float hi) {
    bf16x2 r; r[0] = (__bf16)lo; r[1] = (__bf16)hi; return __builtin_bit_cast(unsigned, r);
}
__device__ __forceinline__ float bflo(unsigned x) { return __uint_as_float(x << 16); }
__device__ __forceinline__ float bfhi(unsigned x) { return __uint_as_float(x & 0xffff0000u); }
__device__ __forceinline__ float dot2(unsigned w, float acc) {
    bf16x2 v = __builtin_bit_cast(bf16x2, w); return __builtin_amdgcn_fdot2_f32_bf16(v, v, acc, false);
}
__device__ __forceinline__ float sumsq9(const u32x4& a, const u32x4& b, unsigned c) {
    float x = dot2(a[0], 0.f), y = dot2(a[1], 0.f), z = dot2(a[2], 0.f);
    x = dot2(a[3], x); y = dot2(b[0], y); z = dot2(b[1], z);
    x = dot2(b[2], x); y = dot2(b[3], y); z = dot2(c, z);
    return x + y + z;
}
__device__ __forceinline__ unsigned scale2(unsigned w, float r) { return cvtpk(bflo(w) * r, bfhi(w) * r); }

#define MFMA16(a, b, c) __builtin_amdgcn_mfma_f32_32x32x16_bf16(__builtin_bit_cast(bf16x8, a), __builtin_bit_cast(bf16x8, b), c, 0, 0, 0)
#define MFMA8(a, b, c) __builtin_amdgcn_mfma_f32_32x32x8bf16_1k(__builtin_bit_cast(s16x4, a), __builtin_bit_cast(s16x4, b), c, 0, 0, 0)
#define SMF(a, b, c) __builtin_amdgcn_smfmac_f32_32x32x32_bf16(__builtin_bit_cast(bf16x8, a), __builtin_bit_cast(bf16x16, b), c, (int)0xDDDDDDDD, 0, 0)
#define LO8(x) __builtin_shufflevector(x, x, 0, 1, 2, 3, 4, 5, 6, 7)
#define HI8(x) __builtin_shufflevector(x, x, 8, 9, 10, 11, 12, 13, 14, 15)

#define EXP4(d, s) \
    "v_exp_f32 v[" #d "+0], v[" #s "+0]\n\t" "v_exp_f32 v[" #d "+1], v[" #s "+1]\n\t" \
    "v_exp_f32 v[" #d "+2], v[" #s "+2]\n\t" "v_exp_f32 v[" #d "+3], v[" #s "+3]\n\t"
#define STEP(C, N, s) \
    "s_waitcnt lgkmcnt(3)\n\t" \
    "v_mfma_f32_32x32x8_bf16 v[" #N ":" #N "+15], v[62:63], v[60:61], 0\n\t" \
    "ds_read_b32 v62, v78 offset:(" #s "+2)*128\n\t" \
    EXP4(C, C) \
    "s_waitcnt lgkmcnt(3)\n\t" \
    "v_mfma_f32_32x32x16_bf16 v[" #N ":" #N "+15], v[64:67], v[56:59], v[" #N ":" #N "+15]\n\t" \
    "ds_read_b128 v[64:67], v77 offset:(" #s "+2)*1024\n\t" \
    EXP4(C + 4, C + 4) \
    "s_waitcnt lgkmcnt(3)\n\t" \
    "v_smfmac_f32_32x32x32_bf16 v[0:15], v[72:75], v[48:55], v76\n\t" \
    "ds_read_b128 v[72:75], v79 offset:(" #s "*4+2)*304\n\t" \
    EXP4(48, C + 8) \
    "s_waitcnt lgkmcnt(3)\n\t" \
    "v_smfmac_f32_32x32x32_bf16 v[0:15], v[68:71], v[" #C ":" #C "+7], v76\n\t" \
    "ds_read_b128 v[68:71], v79 offset:(" #s "*4+4)*304\n\t" \
    EXP4(52, C + 12)

#define STAMP(i)

__global__ __launch_bounds__(1024) void qknorm_attn(const uint16_t* __restrict__ Q, const uint16_t* __restrict__ K,
                                                    const uint16_t* __restrict__ V, const uint16_t* __restrict__ T,
                                                    uint16_t* __restrict__ O
                                                    ) {
    extern __shared__ __attribute__((aligned(16))) char smem[];
    const lds_ptr lds = (lds_ptr)smem;
    const int tid = threadIdx.x, lane = tid & 63, r32 = lane & 31, hi = lane >> 5;
    const int wid = __builtin_amdgcn_readfirstlane(tid >> 6);
    const int bid = blockIdx.x;
    const int bh = (bid & 7) + 8 * (bid >> 5), qc = (bid >> 3) & 3;
    const size_t hb = (size_t)bh * SEQ * DH;
    const bool isV = (wid & 4) != 0;
    const int krow = ((wid & 3) + ((wid >> 3) << 2)) * 64 + lane;
    const char* src = (const char*)((isV ? V : K) + hb) + krow * 36;

    u32x4 ra = *(const u32x4_a4*)(src), rb = *(const u32x4_a4*)(src + 16);
    unsigned rc = *(const unsigned*)(src + 32);

    const char* qp = (const char*)(Q + hb) + (qc * 512 + wid * 32 + r32) * 36;
    const u32x4 qa = *(const u32x4_a4*)(qp), qb = *(const u32x4_a4*)(qp + 16);
    const unsigned qcw = *(const unsigned*)(qp + 32);
    const float tval = __uint_as_float((unsigned)T[bh & (NHEAD - 1)] << 16);
    const float cs = tval * LOG2E;
    const bool fast = __builtin_fabsf(cs) <= FAST_LIMIT;
    u32x4 qm; u32x2 qe;
    {
        const float r = cs * __builtin_amdgcn_rcpf(__builtin_amdgcn_sqrtf(sumsq9(qa, qb, qcw)) + EPS);
        const u32x4 qsel = hi ? qb : qa;
#pragma unroll
        for (int j = 0; j < 4; ++j) qm[j] = scale2(qsel[j], r);
        const float shift = fast ? -(__builtin_fabsf(cs) * 1.015625f) : 0.f;
        qe[0] = hi ? 0u : scale2(qcw, r);
        qe[1] = hi ? 0u : cvtpk(shift, 0.f);
    }
    STAMP(1);

    const lds_cptr kmp = (lds_cptr)lds + LDS_KM + lane * 16;
    const lds_cptr kep = (lds_cptr)lds + LDS_KE + r32 * 4;
    const lds_cptr vtp = (lds_cptr)lds + LDS_VT + (hi * 19 + r32) * 16;
#define LDKM(t) (*(const __attribute__((address_space(3))) u32x4*)(kmp + (t) * 1024))
#define LDKE(t) (*(const __attribute__((address_space(3))) unsigned*)(kep + (t) * 128))
#define LDVT(t, s) (*(const __attribute__((address_space(3))) u32x4*)(vtp + ((t) * 4 + (s) * 2) * VBLK))
    f32x16 o = {}, sA = {}, sB = {};
    f32x8 ph = {};
    u32x2 ke; ke[0] = 0; ke[1] = 0x00003F80u;
    u32x4 km = {}, v0 = {}, v1 = {};
    unsigned aKM = (unsigned)(uintptr_t)kmp - 2 * 1024, aKE = (unsigned)(uintptr_t)kep - 2 * 128, aVT = (unsigned)(uintptr_t)vtp - 2 * 4 * VBLK;
    const f32x16 zero = {};

#pragma unroll 1
    for (int p = 0; p < 4; ++p) {
        const int key = p * 512 + krow;
        if (!isV) {
            const float r = __builtin_amdgcn_rcpf(__builtin_amdgcn_sqrtf(sumsq9(ra, rb, rc)) + EPS);
            u32x4 a, b;
#pragma unroll
            for (int j = 0; j < 4; ++j) { a[j] = scale2(ra[j], r); b[j] = scale2(rb[j], r); }
            const unsigned e = scale2(rc, r);
            lds_ptr pk = lds + LDS_KM + (key >> 5) * 1024 + (key & 31) * 16;
            *(__attribute__((address_space(3))) u32x4*)(pk) = a;
            *(__attribute__((address_space(3))) u32x4*)(pk + 512) = b;
            *(__attribute__((address_space(3))) unsigned*)(lds + LDS_KE + key * 4) = e;
        } else {
            lds_ptr pv = lds + LDS_VT + (key >> 3) * VBLK + (key & 7) * 2;
#define WR16(off, val) *(__attribute__((address_space(3))) uint16_t*)(pv + (off)) = (uint16_t)(val)
#pragma unroll
            for (int j = 0; j < 4; ++j) {
                WR16((2 * j) * 16, ra[j]); WR16((2 * j + 1) * 16, ra[j] >> 16);
                WR16((8 + 2 * j) * 16, rb[j]); WR16((8 + 2 * j + 1) * 16, rb[j] >> 16);
            }
            WR16(16 * 16, rc); WR16(17 * 16, rc >> 16); WR16(18 * 16, 0x3F80u);
        }
        if (p < 3) {
            src += 512 * 36;
            ra = *(const u32x4_a4*)(src); rb = *(const u32x4_a4*)(src + 16); rc = *(const unsigned*)(src + 32);
        }
        asm volatile("s_waitcnt lgkmcnt(0)\n\ts_barrier" ::: "memory");
        if (p == 0) { STAMP(2); }
        if (fast) {
            if (p == 0) {
                ke[0] = LDKE(0);
                sA = MFMA8(ke, qe, zero); sA = MFMA16(LDKM(0), qm, sA);
                ke[0] = LDKE(1); km = LDKM(1); v0 = LDVT(0, 0);
            }
            int cnt = 2;
            asm volatile(
                "s_waitcnt lgkmcnt(0)\n\t"
                "s_nop 7\n\t"
                "s_cmp_eq_u32 %[p], 0\n\t"
                "s_cbranch_scc1 Lent2_%=\n\t"
                "Lloop_%=:\n\t"
                STEP(16, 32, 0) STEP(32, 16, 1)
                "s_cmp_eq_u32 %[cnt], 0\n\t"
                "s_cbranch_scc1 Ldone_%=\n\t"
                "Lent2_%=:\n\t"
                STEP(16, 32, 2) STEP(32, 16, 3) STEP(16, 32, 4) STEP(32, 16, 5) STEP(16, 32, 6) STEP(32, 16, 7)
                "v_add_u32 v77, 8*1024, v77\n\t"
                "v_add_u32 v78, 8*128, v78\n\t"
                "v_add_u32 v79, 8*4*304, v79\n\t"
                "s_sub_u32 %[cnt], %[cnt], 1\n\t"
                "s_cmp_lg_u32 %[cnt], 0\n\t"
                "s_cbranch_scc1 Lloop_%=\n\t"
                "s_cmp_eq_u32 %[p], 3\n\t"
                "s_cbranch_scc1 Lloop_%=\n\t"
                "Ldone_%=:\n\t"
                "s_waitcnt lgkmcnt(0)\n\t"
                "s_nop 7\n\t"
                : "+{v[0:15]}"(o), "+{v[16:31]}"(sA), "+{v[32:47]}"(sB), "+{v[48:55]}"(ph), "+{v[62:63]}"(ke), "+{v[64:67]}"(km),
                  "+{v[68:71]}"(v0), "+{v[72:75]}"(v1), "+{v77}"(aKM), "+{v78}"(aKE), "+{v79}"(aVT), [cnt] "+s"(cnt)
                : "{v[56:59]}"(qm), "{v[60:61]}"(qe), "{v76}"(0xDDDDDDDDu), [p] "s"(p)
                : "memory", "scc");
        }
        if (p == 0) { STAMP(3); }
    }
    if (fast) {
        o = SMF(v1, ph, o);
    } else {
        float m = -INFINITY;
        for (int t = 0; t < NTILE; ++t) {
            f32x16 s;
            ke[0] = LDKE(t); s = MFMA8(ke, qe, zero); s = MFMA16(LDKM(t), qm, s);
            float mx = s[0];
#pragma unroll
            for (int r = 1; r < 16; ++r) mx = __builtin_fmaxf(mx, s[r]);
            mx = __builtin_fmaxf(mx, __shfl_xor(mx, 32));
            const float mn = __builtin_fmaxf(m, mx);
            const float alpha = __builtin_amdgcn_exp2f(m - mn);
            m = mn;
#pragma unroll
            for (int r = 0; r < 16; ++r) { o[r] *= alpha; s[r] = __builtin_amdgcn_exp2f(s[r] - mn); }
            o = SMF(LDVT(t, 0), LO8(s), o);
            o = SMF(LDVT(t, 1), HI8(s), o);
        }
    }
    STAMP(4);
    {
        const float l = __shfl(o[10], r32);
        const float inv = 1.0f / l;
        char* op = (char*)(O + hb) + (qc * 512 + wid * 32 + r32) * 36 + hi * 8;
        u32x2 a, b;
        a[0] = cvtpk(o[0] * inv, o[1] * inv); a[1] = cvtpk(o[2] * inv, o[3] * inv);
        b[0] = cvtpk(o[4] * inv, o[5] * inv); b[1] = cvtpk(o[6] * inv, o[7] * inv);
        *(u32x2_a4*)(op) = a;
        *(u32x2_a4*)(op + 16) = b;
        if (!hi) *(unsigned*)(op + 32) = cvtpk(o[8] * inv, o[9] * inv);
    }
}

extern "C" void kernel_launch(void* const* d_in, const int* in_sizes, int n_in,
                              void* d_out, int out_size, void* d_ws, size_t ws_size,
                              hipStream_t stream) {
    (void)in_sizes; (void)n_in; (void)out_size; (void)d_ws; (void)ws_size;
    (void)hipFuncSetAttribute((const void*)qknorm_attn, hipFuncAttributeMaxDynamicSharedMemorySize, LDS_BYTES);
    qknorm_attn<<<NBH * 4, 1024, LDS_BYTES, stream>>>((const uint16_t*)d_in[0], (const uint16_t*)d_in[1],
                                                      (const uint16_t*)d_in[2], (const uint16_t*)d_in[3],
                                                      (uint16_t*)d_out
                                                      );
}
```

```cpp
#include <hip/hip_runtime.h>
#include <hip/hip_bf16.h>
#include <stdint.h>

typedef float f32x32 __attribute__((ext_vector_type(32)));
typedef float f32x16 __attribute__((ext_vector_type(16)));
typedef float f32x8 __attribute__((ext_vector_type(8)));
typedef __bf16 bf16x8 __attribute__((ext_vector_type(8)));
typedef __bf16 bf16x16 __attribute__((ext_vector_type(16)));
typedef __bf16 bf16x2 __attribute__((ext_vector_type(2)));
typedef short s16x4 __attribute__((ext_vector_type(4)));
typedef unsigned u32x4 __attribute__((ext_vector_type(4)));
typedef unsigned u32x2 __attribute__((ext_vector_type(2)));
typedef u32x4 u32x4_a4 __attribute__((aligned(4)));

constexpr int SEQ = 2048, DH = 18, NHEAD = 32, NBH = 64;
constexpr int NTILE = SEQ / 32;
constexpr int VBLK = 19 * 16;
constexpr int LDS_KE = 256;
constexpr int LDS_KM = LDS_KE + SEQ * 4;
constexpr int LDS_VT = LDS_KM + SEQ * 32;
constexpr int LDS_END = LDS_VT + (SEQ / 8) * VBLK;
constexpr int LDS_BYTES = LDS_END + 2048;
constexpr float LOG2E = 1.4426950408889634f;
constexpr float EPS = 1e-6f;
constexpr float FAST_LIMIT = 16.0f;

typedef __attribute__((address_space(3))) char* lds_ptr;
typedef __attribute__((address_space(3))) const char* lds_cptr;

__device__ __forceinline__ unsigned cvtpk(float lo, float hi) {
    bf16x2 r; r[0] = (__bf16)lo; r[1] = (__bf16)hi; return __builtin_bit_cast(unsigned, r);
}
__device__ __forceinline__ float bflo(unsigned x) { return __uint_as_float(x << 16); }
__device__ __forceinline__ float bfhi(unsigned x) { return __uint_as_float(x & 0xffff0000u); }
__device__ __forceinline__ float dot2(unsigned w, float acc) {
    bf16x2 v = __builtin_bit_cast(bf16x2, w); return __builtin_amdgcn_fdot2_f32_bf16(v, v, acc, false);
}
__device__ __forceinline__ float sumsq9(const u32x4& a, const u32x4& b, unsigned c) {
    float x = dot2(a[0], 0.f), y = dot2(a[1], 0.f), z = dot2(a[2], 0.f);
    x = dot2(a[3], x); y = dot2(b[0], y); z = dot2(b[1], z);
    x = dot2(b[2], x); y = dot2(b[3], y); z = dot2(c, z);
    return x + y + z;
}
__device__ __forceinline__ unsigned scale2(unsigned w, float r) { return cvtpk(bflo(w) * r, bfhi(w) * r); }

#define MFMA16(a, b, c) __builtin_amdgcn_mfma_f32_32x32x16_bf16(__builtin_bit_cast(bf16x8, a), __builtin_bit_cast(bf16x8, b), c, 0, 0, 0)
#define MFMA4(a, b, c) __builtin_amdgcn_mfma_f32_32x32x4bf16_1k(__builtin_bit_cast(s16x4, a), __builtin_bit_cast(s16x4, b), c, 0, 0, 0)
#define SMF(a, b, c) __builtin_amdgcn_smfmac_f32_32x32x32_bf16(__builtin_bit_cast(bf16x8, a), __builtin_bit_cast(bf16x16, b), c, (int)0xDDDDDDDD, 0, 0)
#define LO8(x) __builtin_shufflevector(x, x, 0, 1, 2, 3, 4, 5, 6, 7)
#define HI8(x) __builtin_shufflevector(x, x, 8, 9, 10, 11, 12, 13, 14, 15)
#define LO16(x) __builtin_shufflevector(x, x, 0, 1, 2, 3, 4, 5, 6, 7, 8, 9, 10, 11, 12, 13, 14, 15)
#define HI16(x) __builtin_shufflevector(x, x, 16, 17, 18, 19, 20, 21, 22, 23, 24, 25, 26, 27, 28, 29, 30, 31)

#define EXP4(d, s) \
    "v_exp_f32 v[" #d "+0], v[" #s "+0]\n\t" "v_exp_f32 v[" #d "+1], v[" #s "+1]\n\t" \
    "v_exp_f32 v[" #d "+2], v[" #s "+2]\n\t" "v_exp_f32 v[" #d "+3], v[" #s "+3]\n\t"
#define STEP(C, N, s) \
    "s_waitcnt lgkmcnt(3)\n\t" \
    "v_mfma_f32_32x32x4_2b_bf16 v[" #N ":" #N "+31], v[122:123], v[120:121], 0\n\t" \
    "ds_read_b32 v122, v138 offset:(" #s "+2)*128\n\t" \
    EXP4(C, C) EXP4(C + 4, C + 4) \
    "s_waitcnt lgkmcnt(3)\n\t" \
    "v_smfmac_f32_32x32x32_bf16 v[0:15], v[132:135], v[96:103], v136\n\t" \
    EXP4(C + 16, C + 16) \
    "v_smfmac_f32_32x32x32_bf16 v[16:31], v[132:135], v[104:111], v136\n\t" \
    "ds_read_b128 v[132:135], v139 offset:(" #s "*4+2)*304\n\t" \
    EXP4(C + 20, C + 20) \
    "s_waitcnt lgkmcnt(3)\n\t" \
    "v_mfma_f32_32x32x16_bf16 v[" #N ":" #N "+15], v[124:127], v[112:115], v[" #N ":" #N "+15]\n\t" \
    EXP4(96, C + 8) \
    "v_mfma_f32_32x32x16_bf16 v[" #N "+16:" #N "+31], v[124:127], v[116:119], v[" #N "+16:" #N "+31]\n\t" \
    "ds_read_b128 v[124:127], v137 offset:(" #s "+2)*1024\n\t" \
    EXP4(100, C + 12) \
    "s_waitcnt lgkmcnt(3)\n\t" \
    "v_smfmac_f32_32x32x32_bf16 v[0:15], v[128:131], v[" #C ":" #C "+7], v136\n\t" \
    EXP4(104, C + 24) \
    "v_smfmac_f32_32x32x32_bf16 v[16:31], v[128:131], v[" #C "+16:" #C "+23], v136\n\t" \
    "ds_read_b128 v[128:131], v139 offset:(" #s "*4+4)*304\n\t" \
    EXP4(108, C + 28)

#define STAMP(i)

__global__ __launch_bounds__(512) void qknorm_attn(const uint16_t* __restrict__ Q, const uint16_t* __restrict__ K,
                                                   const uint16_t* __restrict__ V, const uint16_t* __restrict__ T,
                                                   uint16_t* __restrict__ O
                                                   ) {
    extern __shared__ __attribute__((aligned(16))) char smem[];
    const lds_ptr lds = (lds_ptr)smem;
    const int tid = threadIdx.x, lane = tid & 63, r32 = lane & 31, hi = lane >> 5;
    const int bid = blockIdx.x;
    const int bh = (bid & 7) + 8 * (bid >> 5), qc = (bid >> 3) & 3;
    const size_t hb = (size_t)bh * SEQ * DH;
    const char* ksrc = (const char*)(K + hb);
    const char* vsrc = (const char*)(V + hb);
    unsigned roff = tid * 36;

    u32x4 ka = *(const u32x4_a4*)(ksrc + roff), kb = *(const u32x4_a4*)(ksrc + roff + 16);
    unsigned kc = *(const unsigned*)(ksrc + roff + 32);
    u32x4 va = *(const u32x4_a4*)(vsrc + roff), vb = *(const u32x4_a4*)(vsrc + roff + 16);
    unsigned vc = *(const unsigned*)(vsrc + roff + 32);
    const char* qp = (const char*)(Q + hb) + qc * 512 * 36;
    const u32x4 qa = *(const u32x4_a4*)(qp + roff), qb = *(const u32x4_a4*)(qp + roff + 16);
    const unsigned qcw = *(const unsigned*)(qp + roff + 32);
    const float tval = __uint_as_float((unsigned)T[bh & (NHEAD - 1)] << 16);
    const float cs = tval * LOG2E;
    const bool fast = __builtin_fabsf(cs) <= FAST_LIMIT;

    const lds_cptr kmp = (lds_cptr)lds + LDS_KM + lane * 16;
    const lds_cptr kep = (lds_cptr)lds + LDS_KE + r32 * 4;
    const lds_cptr vtp = (lds_cptr)lds + LDS_VT + (hi * 19 + r32) * 16;
#define LDKM(t) (*(const __attribute__((address_space(3))) u32x4*)(kmp + (t) * 1024))
#define LDKE(t) (*(const __attribute__((address_space(3))) unsigned*)(kep + (t) * 128))
#define LDVT(t, s) (*(const __attribute__((address_space(3))) u32x4*)(vtp + ((t) * 4 + (s) * 2) * VBLK))
    f32x16 oa = {}, ob = {};
    f32x32 s0, s1;
    f32x8 pha = {}, phb = {};
    u32x2 ke; ke[0] = 0; ke[1] = 0x00003F80u;
    u32x4 km, v0, v1 = {};
    u32x4 qma, qmb; u32x2 qe;
    unsigned aKM = (unsigned)(uintptr_t)kmp - 2 * 1024, aKE = (unsigned)(uintptr_t)kep - 2 * 128;
    unsigned aVT = r32 < 19 ? (unsigned)(uintptr_t)vtp - 2 * 4 * VBLK : 0x40000u;
    unsigned wK = (unsigned)(uintptr_t)(lds + LDS_KM + (tid >> 5) * 1024 + (tid & 31) * 16);
    unsigned wE = (unsigned)(uintptr_t)(lds + LDS_KE + tid * 4);
    unsigned wV = (unsigned)(uintptr_t)(lds + LDS_VT + (tid >> 3) * VBLK + (tid & 7) * 2);

#pragma unroll 1
    for (int p = 0; p < 4; ++p) {
        {
            const float r = __builtin_amdgcn_rcpf(__builtin_amdgcn_sqrtf(sumsq9(ka, kb, kc)) + EPS);
            u32x4 a, b;
#pragma unroll
            for (int j = 0; j < 4; ++j) { a[j] = scale2(ka[j], r); b[j] = scale2(kb[j], r); }
            const unsigned e = scale2(kc, r);
            asm volatile("" : "+v"(wK), "+v"(wE), "+v"(wV));
            *(__attribute__((address_space(3))) u32x4*)(wK) = a;
            *(__attribute__((address_space(3))) u32x4*)(wK + 512) = b;
            *(__attribute__((address_space(3))) unsigned*)(wE) = e;
#define WR16(off, val) *(__attribute__((address_space(3))) uint16_t*)(wV + (off)) = (uint16_t)(val)
#pragma unroll
            for (int j = 0; j < 4; ++j) {
                WR16((2 * j) * 16, va[j]); WR16((2 * j + 1) * 16, va[j] >> 16);
                WR16((8 + 2 * j) * 16, vb[j]); WR16((8 + 2 * j + 1) * 16, vb[j] >> 16);
            }
            WR16(16 * 16, vc); WR16(17 * 16, vc >> 16); WR16(18 * 16, 0x3F80u);
            wK += 16 * 1024; wE += 512 * 4; wV += 64 * VBLK;
        }
        if (p < 3) {
            roff += 512 * 36;
            ka = *(const u32x4_a4*)(ksrc + roff); kb = *(const u32x4_a4*)(ksrc + roff + 16); kc = *(const unsigned*)(ksrc + roff + 32);
            va = *(const u32x4_a4*)(vsrc + roff); vb = *(const u32x4_a4*)(vsrc + roff + 16); vc = *(const unsigned*)(vsrc + roff + 32);
        }
        if (p == 0) {
            const float r = cs * __builtin_amdgcn_rcpf(__builtin_amdgcn_sqrtf(sumsq9(qa, qb, qcw)) + EPS);
#pragma unroll
            for (int j = 0; j < 4; ++j) {
                auto sw = __builtin_amdgcn_permlane32_swap(scale2(qa[j], r), scale2(qb[j], r), false, false);
                qma[j] = sw[0]; qmb[j] = sw[1];
            }
            const float shift = fast ? -(__builtin_fabsf(cs) * 1.015625f) : 0.f;
            qe[0] = scale2(qcw, r);
            qe[1] = cvtpk(shift, 0.f);
            STAMP(1);
        }
        asm volatile("s_waitcnt lgkmcnt(0)\n\ts_barrier" ::: "memory");
        if (p == 0) { STAMP(2); }
        if (fast) {
            if (p == 0) {
                ke[0] = LDKE(0);
                s0 = MFMA4(ke, qe, (f32x32){});
                const u32x4 k0 = LDKM(0);
                f32x16 sa = MFMA16(k0, qma, LO16(s0)), sb = MFMA16(k0, qmb, HI16(s0));
                s0 = __builtin_shufflevector(sa, sb, 0, 1, 2, 3, 4, 5, 6, 7, 8, 9, 10, 11, 12, 13, 14, 15, 16, 17, 18, 19, 20, 21, 22, 23, 24, 25, 26, 27, 28, 29, 30, 31);
                ke[0] = LDKE(1); km = LDKM(1);
                v0 = *(const __attribute__((address_space(3))) u32x4*)(aVT + 2 * 4 * VBLK);
            }
            int cnt = 4;
            asm volatile(
                "s_waitcnt lgkmcnt(0)\n\t"
                "s_nop 7\n\t"
                "s_cmp_eq_u32 %[p], 0\n\t"
                "s_cbranch_scc1 Lent2_%=\n\t"
                "Lloop_%=:\n\t"
                STEP(32, 64, 0) STEP(64, 32, 1)
                "s_cmp_eq_u32 %[cnt], 0\n\t"
                "s_cbranch_scc1 Ldone_%=\n\t"
                "Lent2_%=:\n\t"
                STEP(32, 64, 2) STEP(64, 32, 3)
                "v_add_u32 v137, 4*1024, v137\n\t"
                "v_add_u32 v138, 4*128, v138\n\t"
                "v_add_u32 v139, 4*4*304, v139\n\t"
                "s_sub_u32 %[cnt], %[cnt], 1\n\t"
                "s_cmp_lg_u32 %[cnt], 0\n\t"
                "s_cbranch_scc1 Lloop_%=\n\t"
                "s_cmp_eq_u32 %[p], 3\n\t"
                "s_cbranch_scc1 Lloop_%=\n\t"
                "Ldone_%=:\n\t"
                "s_waitcnt lgkmcnt(0)\n\t"
                "s_nop 7\n\t"
                : "+{v[0:15]}"(oa), "+{v[16:31]}"(ob), "+{v[32:63]}"(s0), "=&{v[64:95]}"(s1), "+{v[96:103]}"(pha), "+{v[104:111]}"(phb),
                  "+{v[122:123]}"(ke), "+{v[124:127]}"(km), "+{v[128:131]}"(v0), "+{v[132:135]}"(v1),
                  "+{v137}"(aKM), "+{v138}"(aKE), "+{v139}"(aVT), [cnt] "+s"(cnt)
                : "{v[112:115]}"(qma), "{v[116:119]}"(qmb), "{v[120:121]}"(qe), "{v136}"(0xDDDDDDDDu), [p] "s"(p)
                : "memory", "scc");
        }
        if (p == 0) { STAMP(3); }
    }
    if (fast) {
        oa = SMF(v1, pha, oa);
        ob = SMF(v1, phb, ob);
    } else {
        float ma = -INFINITY, mb = -INFINITY;
        for (int t = 0; t < NTILE; ++t) {
            ke[0] = LDKE(t);
            const f32x32 s = MFMA4(ke, qe, (f32x32){});
            const u32x4 k0 = LDKM(t);
            f32x16 sa = MFMA16(k0, qma, LO16(s)), sb = MFMA16(k0, qmb, HI16(s));
            float xa = sa[0], xb = sb[0];
#pragma unroll
            for (int r = 1; r < 16; ++r) { xa = __builtin_fmaxf(xa, sa[r]); xb = __builtin_fmaxf(xb, sb[r]); }
            xa = __builtin_fmaxf(xa, __shfl_xor(xa, 32)); xb = __builtin_fmaxf(xb, __shfl_xor(xb, 32));
            const float na = __builtin_fmaxf(ma, xa), nb = __builtin_fmaxf(mb, xb);
            const float fa = __builtin_amdgcn_exp2f(ma - na), fb = __builtin_amdgcn_exp2f(mb - nb);
            ma = na; mb = nb;
#pragma unroll
            for (int r = 0; r < 16; ++r) {
                oa[r] *= fa; sa[r] = __builtin_amdgcn_exp2f(sa[r] - na);
                ob[r] *= fb; sb[r] = __builtin_amdgcn_exp2f(sb[r] - nb);
            }
            const u32x4 w0 = LDVT(t, 0), w1 = LDVT(t, 1);
            oa = SMF(w0, LO8(sa), oa); ob = SMF(w0, LO8(sb), ob);
            oa = SMF(w1, HI8(sa), oa); ob = SMF(w1, HI8(sb), ob);
        }
    }
    STAMP(4);
    {
        float x[11], y[8];
#pragma unroll
        for (int r = 0; r < 11; ++r) {
            auto sw = __builtin_amdgcn_permlane32_swap(__float_as_uint(oa[r]), __float_as_uint(ob[r]), false, false);
            x[r] = __uint_as_float(sw[0]); if (r < 8) y[r] = __uint_as_float(sw[1]);
        }
        const float inv = __builtin_amdgcn_rcpf(x[10]);
        u32x4 a, b;
        a[0] = cvtpk(x[0] * inv, x[1] * inv); a[1] = cvtpk(x[2] * inv, x[3] * inv);
        a[2] = cvtpk(y[0] * inv, y[1] * inv); a[3] = cvtpk(y[2] * inv, y[3] * inv);
        b[0] = cvtpk(x[4] * inv, x[5] * inv); b[1] = cvtpk(x[6] * inv, x[7] * inv);
        b[2] = cvtpk(y[4] * inv, y[5] * inv); b[3] = cvtpk(y[6] * inv, y[7] * inv);
        char* op = (char*)(O + hb) + (qc * 512 + tid) * 36;
        *(u32x4_a4*)(op) = a;
        *(u32x4_a4*)(op + 16) = b;
        *(unsigned*)(op + 32) = cvtpk(x[8] * inv, x[9] * inv);
    }
}

extern "C" void kernel_launch(void* const* d_in, const int* in_sizes, int n_in,
                              void* d_out, int out_size, void* d_ws, size_t ws_size,
                              hipStream_t stream) {
    (void)in_sizes; (void)n_in; (void)out_size; (void)d_ws; (void)ws_size;
    (void)hipFuncSetAttribute((const void*)qknorm_attn, hipFuncAttributeMaxDynamicSharedMemorySize, LDS_BYTES);
    qknorm_attn<<<NBH * 4, 512, LDS_BYTES, stream>>>((const uint16_t*)d_in[0], (const uint16_t*)d_in[1],
                                                     (const uint16_t*)d_in[2], (const uint16_t*)d_in[3],
                                                     (uint16_t*)d_out
                                                     );
}
```

```cpp
#include <hip/hip_runtime.h>
#include <hip/hip_bf16.h>
#include <stdint.h>

typedef float f32x32 __attribute__((ext_vector_type(32)));
typedef float f32x16 __attribute__((ext_vector_type(16)));
typedef float f32x8 __attribute__((ext_vector_type(8)));
typedef __bf16 bf16x8 __attribute__((ext_vector_type(8)));
typedef __bf16 bf16x16 __attribute__((ext_vector_type(16)));
typedef __bf16 bf16x2 __attribute__((ext_vector_type(2)));
typedef short s16x4 __attribute__((ext_vector_type(4)));
typedef unsigned u32x4 __attribute__((ext_vector_type(4)));
typedef unsigned u32x2 __attribute__((ext_vector_type(2)));
typedef u32x4 u32x4_a4 __attribute__((aligned(4)));

constexpr int SEQ = 2048, DH = 18, NHEAD = 32, NBH = 64;
constexpr int NTILE = SEQ / 32;
constexpr int VBLK = 19 * 16;
constexpr int LDS_KE = 256;
constexpr int LDS_KM = LDS_KE + SEQ * 4;
constexpr int LDS_VT = LDS_KM + SEQ * 32;
constexpr int LDS_END = LDS_VT + (SEQ / 8) * VBLK;
constexpr int LDS_BYTES = LDS_END + 2048;
constexpr float LOG2E = 1.4426950408889634f;
constexpr float EPS = 1e-6f;
constexpr float FAST_LIMIT = 16.0f;

typedef __attribute__((address_space(3))) char* lds_ptr;
typedef __attribute__((address_space(3))) const char* lds_cptr;

__device__ __forceinline__ unsigned cvtpk(float lo, float hi) {
    bf16x2 r; r[0] = (__bf16)lo; r[1] = (__bf16)hi; return __builtin_bit_cast(unsigned, r);
}
__device__ __forceinline__ float bflo(unsigned x) { return __uint_as_float(x << 16); }
__device__ __forceinline__ float bfhi(unsigned x) { return __uint_as_float(x & 0xffff0000u); }
__device__ __forceinline__ float dot2(unsigned w, float acc) {
    bf16x2 v = __builtin_bit_cast(bf16x2, w); return __builtin_amdgcn_fdot2_f32_bf16(v, v, acc, false);
}
__device__ __forceinline__ float sumsq9(const u32x4& a, const u32x4& b, unsigned c) {
    float x = dot2(a[0], 0.f), y = dot2(a[1], 0.f), z = dot2(a[2], 0.f);
    x = dot2(a[3], x); y = dot2(b[0], y); z = dot2(b[1], z);
    x = dot2(b[2], x); y = dot2(b[3], y); z = dot2(c, z);
    return x + y + z;
}
__device__ __forceinline__ unsigned scale2(unsigned w, float r) { return cvtpk(bflo(w) * r, bfhi(w) * r); }

#define MFMA16(a, b, c) __builtin_amdgcn_mfma_f32_32x32x16_bf16(__builtin_bit_cast(bf16x8, a), __builtin_bit_cast(bf16x8, b), c, 0, 0, 0)
#define MFMA4(a, b, c) __builtin_amdgcn_mfma_f32_32x32x4bf16_1k(__builtin_bit_cast(s16x4, a), __builtin_bit_cast(s16x4, b), c, 0, 0, 0)
#define SMF(a, b, c) __builtin_amdgcn_smfmac_f32_32x32x32_bf16(__builtin_bit_cast(bf16x8, a), __builtin_bit_cast(bf16x16, b), c, (int)0xDDDDDDDD, 0, 0)
#define LO8(x) __builtin_shufflevector(x, x, 0, 1, 2, 3, 4, 5, 6, 7)
#define HI8(x) __builtin_shufflevector(x, x, 8, 9, 10, 11, 12, 13, 14, 15)
#define LO16(x) __builtin_shufflevector(x, x, 0, 1, 2, 3, 4, 5, 6, 7, 8, 9, 10, 11, 12, 13, 14, 15)
#define HI16(x) __builtin_shufflevector(x, x, 16, 17, 18, 19, 20, 21, 22, 23, 24, 25, 26, 27, 28, 29, 30, 31)

#define EXP4(d, s) \
    "v_exp_f32 v[" #d "+0], v[" #s "+0]\n\t" "v_exp_f32 v[" #d "+1], v[" #s "+1]\n\t" \
    "v_exp_f32 v[" #d "+2], v[" #s "+2]\n\t" "v_exp_f32 v[" #d "+3], v[" #s "+3]\n\t"
#define STEP(C, N, s) \
    "s_waitcnt lgkmcnt(3)\n\t" \
    "v_mfma_f32_32x32x4_2b_bf16 v[" #N ":" #N "+31], v[122:123], v[120:121], 0\n\t" \
    "ds_read_b32 v122, v138 offset:(" #s "+2)*128\n\t" \
    EXP4(C, C) EXP4(C + 4, C + 4) \
    "s_waitcnt lgkmcnt(3)\n\t" \
    "v_smfmac_f32_32x32x32_bf16 v[0:15], v[132:135], v[96:103], v136\n\t" \
    EXP4(C + 16, C + 16) \
    "v_smfmac_f32_32x32x32_bf16 v[16:31], v[132:135], v[104:111], v136\n\t" \
    "ds_read_b128 v[132:135], v139 offset:(" #s "*4+2)*304\n\t" \
    EXP4(C + 20, C + 20) \
    "s_waitcnt lgkmcnt(3)\n\t" \
    "v_mfma_f32_32x32x16_bf16 v[" #N ":" #N "+15], v[124:127], v[112:115], v[" #N ":" #N "+15]\n\t" \
    EXP4(96, C + 8) \
    "v_mfma_f32_32x32x16_bf16 v[" #N "+16:" #N "+31], v[124:127], v[116:119], v[" #N "+16:" #N "+31]\n\t" \
    "ds_read_b128 v[124:127], v137 offset:(" #s "+2)*1024\n\t" \
    EXP4(100, C + 12) \
    "s_waitcnt lgkmcnt(3)\n\t" \
    "v_smfmac_f32_32x32x32_bf16 v[0:15], v[128:131], v[" #C ":" #C "+7], v136\n\t" \
    EXP4(104, C + 24) \
    "v_smfmac_f32_32x32x32_bf16 v[16:31], v[128:131], v[" #C "+16:" #C "+23], v136\n\t" \
    "ds_read_b128 v[128:131], v139 offset:(" #s "*4+4)*304\n\t" \
    EXP4(108, C + 28)

#define STAMP(i)

__global__ __launch_bounds__(512) void qknorm_attn(const uint16_t* __restrict__ Q, const uint16_t* __restrict__ K,
                                                   const uint16_t* __restrict__ V, const uint16_t* __restrict__ T,
                                                   uint16_t* __restrict__ O
                                                   ) {
    extern __shared__ __attribute__((aligned(16))) char smem[];
    const unsigned long long rt0_ = __builtin_amdgcn_s_memrealtime();
    const lds_ptr lds = (lds_ptr)smem;
    const int tid = threadIdx.x, lane = tid & 63, r32 = lane & 31, hi = lane >> 5;
    const int bid = blockIdx.x;
    const int bh = (bid & 7) + 8 * (bid >> 5), qc = (bid >> 3) & 3;
    const size_t hb = (size_t)bh * SEQ * DH;
    const char* ksrc = (const char*)(K + hb);
    const char* vsrc = (const char*)(V + hb);
    unsigned roff = tid * 36;

    u32x4 ka = *(const u32x4_a4*)(ksrc + roff), kb = *(const u32x4_a4*)(ksrc + roff + 16);
    unsigned kc = *(const unsigned*)(ksrc + roff + 32);
    u32x4 va = *(const u32x4_a4*)(vsrc + roff), vb = *(const u32x4_a4*)(vsrc + roff + 16);
    unsigned vc = *(const unsigned*)(vsrc + roff + 32);
    const char* qp = (const char*)(Q + hb) + qc * 512 * 36;
    const u32x4 qa = *(const u32x4_a4*)(qp + roff), qb = *(const u32x4_a4*)(qp + roff + 16);
    const unsigned qcw = *(const unsigned*)(qp + roff + 32);
    const float tval = __uint_as_float((unsigned)T[bh & (NHEAD - 1)] << 16);
    const float cs = tval * LOG2E;
    const bool fast = __builtin_fabsf(cs) <= FAST_LIMIT;

    const lds_cptr kmp = (lds_cptr)lds + LDS_KM + lane * 16;
    const lds_cptr kep = (lds_cptr)lds + LDS_KE + r32 * 4;
    const lds_cptr vtp = (lds_cptr)lds + LDS_VT + (hi * 19 + r32) * 16;
#define LDKM(t) (*(const __attribute__((address_space(3))) u32x4*)(kmp + (t) * 1024))
#define LDKE(t) (*(const __attribute__((address_space(3))) unsigned*)(kep + (t) * 128))
#define LDVT(t, s) (*(const __attribute__((address_space(3))) u32x4*)(vtp + ((t) * 4 + (s) * 2) * VBLK))
    f32x16 oa = {}, ob = {};
    f32x32 s0, s1;
    f32x8 pha = {}, phb = {};
    u32x2 ke; ke[0] = 0; ke[1] = 0x00003F80u;
    u32x4 km, v0, v1 = {};
    u32x4 qma, qmb; u32x2 qe;
    unsigned aKM = (unsigned)(uintptr_t)kmp - 2 * 1024, aKE = (unsigned)(uintptr_t)kep - 2 * 128;
    unsigned aVT = r32 < 19 ? (unsigned)(uintptr_t)vtp - 2 * 4 * VBLK : 0x40000u;
    unsigned wK = (unsigned)(uintptr_t)(lds + LDS_KM + (tid >> 5) * 1024 + (tid & 31) * 16);
    unsigned wE = (unsigned)(uintptr_t)(lds + LDS_KE + tid * 4);
    unsigned wV = (unsigned)(uintptr_t)(lds + LDS_VT + (tid >> 3) * VBLK + (tid & 7) * 2);

#pragma unroll 1
    for (int p = 0; p < 4; ++p) {
        {
            const float r = __builtin_amdgcn_rcpf(__builtin_amdgcn_sqrtf(sumsq9(ka, kb, kc)) + EPS);
            u32x4 a, b;
#pragma unroll
            for (int j = 0; j < 4; ++j) { a[j] = scale2(ka[j], r); b[j] = scale2(kb[j], r); }
            const unsigned e = scale2(kc, r);
            asm volatile("" : "+v"(wK), "+v"(wE), "+v"(wV));
            *(__attribute__((address_space(3))) u32x4*)(wK) = a;
            *(__attribute__((address_space(3))) u32x4*)(wK + 512) = b;
            *(__attribute__((address_space(3))) unsigned*)(wE) = e;
#define WR16(off, val) *(__attribute__((address_space(3))) uint16_t*)(wV + (off)) = (uint16_t)(val)
#pragma unroll
            for (int j = 0; j < 4; ++j) {
                WR16((2 * j) * 16, va[j]); WR16((2 * j + 1) * 16, va[j] >> 16);
                WR16((8 + 2 * j) * 16, vb[j]); WR16((8 + 2 * j + 1) * 16, vb[j] >> 16);
            }
            WR16(16 * 16, vc); WR16(17 * 16, vc >> 16); WR16(18 * 16, 0x3F80u);
            wK += 16 * 1024; wE += 512 * 4; wV += 64 * VBLK;
        }
        if (p < 3) {
            roff += 512 * 36;
            ka = *(const u32x4_a4*)(ksrc + roff); kb = *(const u32x4_a4*)(ksrc + roff + 16); kc = *(const unsigned*)(ksrc + roff + 32);
            va = *(const u32x4_a4*)(vsrc + roff); vb = *(const u32x4_a4*)(vsrc + roff + 16); vc = *(const unsigned*)(vsrc + roff + 32);
        }
        if (p == 0) {
            const float r = cs * __builtin_amdgcn_rcpf(__builtin_amdgcn_sqrtf(sumsq9(qa, qb, qcw)) + EPS);
#pragma unroll
            for (int j = 0; j < 4; ++j) {
                auto sw = __builtin_amdgcn_permlane32_swap(scale2(qa[j], r), scale2(qb[j], r), false, false);
                qma[j] = sw[0]; qmb[j] = sw[1];
            }
            const float shift = fast ? -(__builtin_fabsf(cs) * 1.015625f) : 0.f;
            qe[0] = scale2(qcw, r);
            qe[1] = cvtpk(shift, 0.f);
            STAMP(1);
        }
        asm volatile("s_waitcnt lgkmcnt(0)\n\ts_barrier" ::: "memory");
        if (p == 0) { STAMP(2); }
        if (fast) {
            if (p == 0) {
                ke[0] = LDKE(0);
                s0 = MFMA4(ke, qe, (f32x32){});
                const u32x4 k0 = LDKM(0);
                f32x16 sa = MFMA16(k0, qma, LO16(s0)), sb = MFMA16(k0, qmb, HI16(s0));
                s0 = __builtin_shufflevector(sa, sb, 0, 1, 2, 3, 4, 5, 6, 7, 8, 9, 10, 11, 12, 13, 14, 15, 16, 17, 18, 19, 20, 21, 22, 23, 24, 25, 26, 27, 28, 29, 30, 31);
                ke[0] = LDKE(1); km = LDKM(1);
                v0 = *(const __attribute__((address_space(3))) u32x4*)(aVT + 2 * 4 * VBLK);
            }
            int cnt = 4;
            asm volatile(
                "s_waitcnt lgkmcnt(0)\n\t"
                "s_nop 7\n\t"
                "s_cmp_eq_u32 %[p], 0\n\t"
                "s_cbranch_scc1 Lent2_%=\n\t"
                "Lloop_%=:\n\t"
                STEP(32, 64, 0) STEP(64, 32, 1)
                "s_cmp_eq_u32 %[cnt], 0\n\t"
                "s_cbranch_scc1 Ldone_%=\n\t"
                "Lent2_%=:\n\t"
                STEP(32, 64, 2) STEP(64, 32, 3)
                "v_add_u32 v137, 4*1024, v137\n\t"
                "v_add_u32 v138, 4*128, v138\n\t"
                "v_add_u32 v139, 4*4*304, v139\n\t"
                "s_sub_u32 %[cnt], %[cnt], 1\n\t"
                "s_cmp_lg_u32 %[cnt], 0\n\t"
                "s_cbranch_scc1 Lloop_%=\n\t"
                "s_cmp_eq_u32 %[p], 3\n\t"
                "s_cbranch_scc1 Lloop_%=\n\t"
                "Ldone_%=:\n\t"
                "s_waitcnt lgkmcnt(0)\n\t"
                "s_nop 7\n\t"
                : "+{v[0:15]}"(oa), "+{v[16:31]}"(ob), "+{v[32:63]}"(s0), "=&{v[64:95]}"(s1), "+{v[96:103]}"(pha), "+{v[104:111]}"(phb),
                  "+{v[122:123]}"(ke), "+{v[124:127]}"(km), "+{v[128:131]}"(v0), "+{v[132:135]}"(v1),
                  "+{v137}"(aKM), "+{v138}"(aKE), "+{v139}"(aVT), [cnt] "+s"(cnt)
                : "{v[112:115]}"(qma), "{v[116:119]}"(qmb), "{v[120:121]}"(qe), "{v136}"(0xDDDDDDDDu), [p] "s"(p)
                : "memory", "scc");
        }
        if (p == 0) { STAMP(3); }
    }
    if (fast) {
        oa = SMF(v1, pha, oa);
        ob = SMF(v1, phb, ob);
    } else {
        float ma = -INFINITY, mb = -INFINITY;
        for (int t = 0; t < NTILE; ++t) {
            ke[0] = LDKE(t);
            const f32x32 s = MFMA4(ke, qe, (f32x32){});
            const u32x4 k0 = LDKM(t);
            f32x16 sa = MFMA16(k0, qma, LO16(s)), sb = MFMA16(k0, qmb, HI16(s));
            float xa = sa[0], xb = sb[0];
#pragma unroll
            for (int r = 1; r < 16; ++r) { xa = __builtin_fmaxf(xa, sa[r]); xb = __builtin_fmaxf(xb, sb[r]); }
            xa = __builtin_fmaxf(xa, __shfl_xor(xa, 32)); xb = __builtin_fmaxf(xb, __shfl_xor(xb, 32));
            const float na = __builtin_fmaxf(ma, xa), nb = __builtin_fmaxf(mb, xb);
            const float fa = __builtin_amdgcn_exp2f(ma - na), fb = __builtin_amdgcn_exp2f(mb - nb);
            ma = na; mb = nb;
#pragma unroll
            for (int r = 0; r < 16; ++r) {
                oa[r] *= fa; sa[r] = __builtin_amdgcn_exp2f(sa[r] - na);
                ob[r] *= fb; sb[r] = __builtin_amdgcn_exp2f(sb[r] - nb);
            }
            const u32x4 w0 = LDVT(t, 0), w1 = LDVT(t, 1);
            oa = SMF(w0, LO8(sa), oa); ob = SMF(w0, LO8(sb), ob);
            oa = SMF(w1, HI8(sa), oa); ob = SMF(w1, HI8(sb), ob);
        }
    }
    STAMP(4);
    {
        float x[11], y[8];
#pragma unroll
        for (int r = 0; r < 11; ++r) {
            auto sw = __builtin_amdgcn_permlane32_swap(__float_as_uint(oa[r]), __float_as_uint(ob[r]), false, false);
            x[r] = __uint_as_float(sw[0]); if (r < 8) y[r] = __uint_as_float(sw[1]);
        }
        const float inv = __builtin_amdgcn_rcpf(x[10]);
        u32x4 a, b;
        a[0] = cvtpk(x[0] * inv, x[1] * inv); a[1] = cvtpk(x[2] * inv, x[3] * inv);
        a[2] = cvtpk(y[0] * inv, y[1] * inv); a[3] = cvtpk(y[2] * inv, y[3] * inv);
        b[0] = cvtpk(x[4] * inv, x[5] * inv); b[1] = cvtpk(x[6] * inv, x[7] * inv);
        b[2] = cvtpk(y[4] * inv, y[5] * inv); b[3] = cvtpk(y[6] * inv, y[7] * inv);
        char* op = (char*)(O + hb) + (qc * 512 + tid) * 36;
        const unsigned c_ = cvtpk(x[8] * inv, x[9] * inv);
        asm volatile("global_store_dwordx4 %0, %1, off sc0 sc1\n\tglobal_store_dwordx4 %0, %2, off offset:16 sc0 sc1\n\tglobal_store_dword %0, %3, off offset:32 sc0 sc1\n\ts_nop 1" :: "v"(op), "v"(a), "v"(b), "v"(c_) : "memory");
    }
    asm volatile("s_waitcnt vmcnt(0)" ::: "memory");
    if (threadIdx.x < 64) asm volatile("buffer_wbl2 sc0 sc1\n\ts_waitcnt vmcnt(0)" ::: "memory");
    { __builtin_amdgcn_sched_barrier(0); while (__builtin_amdgcn_s_memrealtime() - rt0_ < 4500ull) { __builtin_amdgcn_s_sleep(1); } __builtin_amdgcn_sched_barrier(0); }
}

extern "C" void kernel_launch(void* const* d_in, const int* in_sizes, int n_in,
                              void* d_out, int out_size, void* d_ws, size_t ws_size,
                              hipStream_t stream) {
    (void)in_sizes; (void)n_in; (void)out_size; (void)d_ws; (void)ws_size;
    (void)hipFuncSetAttribute((const void*)qknorm_attn, hipFuncAttributeMaxDynamicSharedMemorySize, LDS_BYTES);
    qknorm_attn<<<NBH * 4, 512, LDS_BYTES, stream>>>((const uint16_t*)d_in[0], (const uint16_t*)d_in[1],
                                                     (const uint16_t*)d_in[2], (const uint16_t*)d_in[3],
                                                     (uint16_t*)d_out
                                                     );
}
```

```cpp
#include <hip/hip_runtime.h>
#include <hip/hip_bf16.h>
#include <stdint.h>

typedef float f32x32 __attribute__((ext_vector_type(32)));
typedef float f32x16 __attribute__((ext_vector_type(16)));
typedef float f32x8 __attribute__((ext_vector_type(8)));
typedef __bf16 bf16x8 __attribute__((ext_vector_type(8)));
typedef __bf16 bf16x16 __attribute__((ext_vector_type(16)));
typedef __bf16 bf16x2 __attribute__((ext_vector_type(2)));
typedef short s16x4 __attribute__((ext_vector_type(4)));
typedef unsigned u32x4 __attribute__((ext_vector_type(4)));
typedef unsigned u32x2 __attribute__((ext_vector_type(2)));
typedef u32x4 u32x4_a4 __attribute__((aligned(4)));

constexpr int SEQ = 2048, DH = 18, NHEAD = 32, NBH = 64;
constexpr int NTILE = SEQ / 32;
constexpr int VBLK = 19 * 16;
constexpr int LDS_KE = 256;
constexpr int LDS_KM = LDS_KE + SEQ * 4;
constexpr int LDS_VT = LDS_KM + SEQ * 32;
constexpr int LDS_END = LDS_VT + (SEQ / 8) * VBLK;
constexpr int LDS_BYTES = LDS_END + 2048;
constexpr float LOG2E = 1.4426950408889634f;
constexpr float EPS = 1e-6f;
constexpr float FAST_LIMIT = 16.0f;

typedef __attribute__((address_space(3))) char* lds_ptr;
typedef __attribute__((address_space(3))) const char* lds_cptr;

__device__ __forceinline__ unsigned cvtpk(float lo, float hi) {
    bf16x2 r; r[0] = (__bf16)lo; r[1] = (__bf16)hi; return __builtin_bit_cast(unsigned, r);
}
__device__ __forceinline__ float bflo(unsigned x) { return __uint_as_float(x << 16); }
__device__ __forceinline__ float bfhi(unsigned x) { return __uint_as_float(x & 0xffff0000u); }
__device__ __forceinline__ float dot2(unsigned w, float acc) {
    bf16x2 v = __builtin_bit_cast(bf16x2, w); return __builtin_amdgcn_fdot2_f32_bf16(v, v, acc, false);
}
__device__ __forceinline__ float sumsq9(const u32x4& a, const u32x4& b, unsigned c) {
    float x = dot2(a[0], 0.f), y = dot2(a[1], 0.f), z = dot2(a[2], 0.f);
    x = dot2(a[3], x); y = dot2(b[0], y); z = dot2(b[1], z);
    x = dot2(b[2], x); y = dot2(b[3], y); z = dot2(c, z);
    return x + y + z;
}
__device__ __forceinline__ unsigned scale2(unsigned w, float r) { return cvtpk(bflo(w) * r, bfhi(w) * r); }

#define MFMA16(a, b, c) __builtin_amdgcn_mfma_f32_32x32x16_bf16(__builtin_bit_cast(bf16x8, a), __builtin_bit_cast(bf16x8, b), c, 0, 0, 0)
#define MFMA4(a, b, c) __builtin_amdgcn_mfma_f32_32x32x4bf16_1k(__builtin_bit_cast(s16x4, a), __builtin_bit_cast(s16x4, b), c, 0, 0, 0)
#define SMF(a, b, c) __builtin_amdgcn_smfmac_f32_32x32x32_bf16(__builtin_bit_cast(bf16x8, a), __builtin_bit_cast(bf16x16, b), c, (int)0xDDDDDDDD, 0, 0)
#define LO8(x) __builtin_shufflevector(x, x, 0, 1, 2, 3, 4, 5, 6, 7)
#define HI8(x) __builtin_shufflevector(x, x, 8, 9, 10, 11, 12, 13, 14, 15)
#define LO16(x) __builtin_shufflevector(x, x, 0, 1, 2, 3, 4, 5, 6, 7, 8, 9, 10, 11, 12, 13, 14, 15)
#define HI16(x) __builtin_shufflevector(x, x, 16, 17, 18, 19, 20, 21, 22, 23, 24, 25, 26, 27, 28, 29, 30, 31)

#define EXP4(d, s) \
    "v_exp_f32 v[" #d "+0], v[" #s "+0]\n\t" "v_exp_f32 v[" #d "+1], v[" #s "+1]\n\t" \
    "v_exp_f32 v[" #d "+2], v[" #s "+2]\n\t" "v_exp_f32 v[" #d "+3], v[" #s "+3]\n\t"
#define STEP(C, N, s) \
    "s_waitcnt lgkmcnt(3)\n\t" \
    "v_mfma_f32_32x32x4_2b_bf16 v[" #N ":" #N "+31], v[122:123], v[120:121], 0\n\t" \
    "ds_read_b32 v122, v138 offset:(" #s "+2)*128\n\t" \
    EXP4(C, C) EXP4(C + 4, C + 4) \
    "s_waitcnt lgkmcnt(3)\n\t" \
    "v_smfmac_f32_32x32x32_bf16 v[0:15], v[132:135], v[96:103], v136\n\t" \
    EXP4(C + 16, C + 16) \
    "v_smfmac_f32_32x32x32_bf16 v[16:31], v[132:135], v[104:111], v136\n\t" \
    "ds_read_b128 v[132:135], v139 offset:(" #s "*4+2)*304\n\t" \
    EXP4(C + 20, C + 20) \
    "s_waitcnt lgkmcnt(3)\n\t" \
    "v_mfma_f32_32x32x16_bf16 v[" #N ":" #N "+15], v[124:127], v[112:115], v[" #N ":" #N "+15]\n\t" \
    EXP4(96, C + 8) \
    "v_mfma_f32_32x32x16_bf16 v[" #N "+16:" #N "+31], v[124:127], v[116:119], v[" #N "+16:" #N "+31]\n\t" \
    "ds_read_b128 v[124:127], v137 offset:(" #s "+2)*1024\n\t" \
    EXP4(100, C + 12) \
    "s_waitcnt lgkmcnt(3)\n\t" \
    "v_smfmac_f32_32x32x32_bf16 v[0:15], v[128:131], v[" #C ":" #C "+7], v136\n\t" \
    EXP4(104, C + 24) \
    "v_smfmac_f32_32x32x32_bf16 v[16:31], v[128:131], v[" #C "+16:" #C "+23], v136\n\t" \
    "ds_read_b128 v[128:131], v139 offset:(" #s "*4+4)*304\n\t" \
    EXP4(108, C + 28)

#define STAMP(i)

__global__ __launch_bounds__(512) void qknorm_attn(const uint16_t* __restrict__ Q, const uint16_t* __restrict__ K,
                                                   const uint16_t* __restrict__ V, const uint16_t* __restrict__ T,
                                                   uint16_t* __restrict__ O, unsigned long long* WS
                                                   ) {
    extern __shared__ __attribute__((aligned(16))) char smem[];
    const unsigned long long rt0_ = __builtin_amdgcn_s_memrealtime();
    if (threadIdx.x == 0) __hip_atomic_store(WS + blockIdx.x, rt0_, __ATOMIC_RELAXED, __HIP_MEMORY_SCOPE_AGENT);
    const lds_ptr lds = (lds_ptr)smem;
    const int tid = threadIdx.x, lane = tid & 63, r32 = lane & 31, hi = lane >> 5;
    const int bid = blockIdx.x;
    const int bh = (bid & 7) + 8 * (bid >> 5), qc = (bid >> 3) & 3;
    const size_t hb = (size_t)bh * SEQ * DH;
    const char* ksrc = (const char*)(K + hb);
    const char* vsrc = (const char*)(V + hb);
    unsigned roff = tid * 36;

    u32x4 ka = *(const u32x4_a4*)(ksrc + roff), kb = *(const u32x4_a4*)(ksrc + roff + 16);
    unsigned kc = *(const unsigned*)(ksrc + roff + 32);
    u32x4 va = *(const u32x4_a4*)(vsrc + roff), vb = *(const u32x4_a4*)(vsrc + roff + 16);
    unsigned vc = *(const unsigned*)(vsrc + roff + 32);
    const char* qp = (const char*)(Q + hb) + qc * 512 * 36;
    const u32x4 qa = *(const u32x4_a4*)(qp + roff), qb = *(const u32x4_a4*)(qp + roff + 16);
    const unsigned qcw = *(const unsigned*)(qp + roff + 32);
    const float tval = __uint_as_float((unsigned)T[bh & (NHEAD - 1)] << 16);
    const float cs = tval * LOG2E;
    const bool fast = __builtin_fabsf(cs) <= FAST_LIMIT;

    const lds_cptr kmp = (lds_cptr)lds + LDS_KM + lane * 16;
    const lds_cptr kep = (lds_cptr)lds + LDS_KE + r32 * 4;
    const lds_cptr vtp = (lds_cptr)lds + LDS_VT + (hi * 19 + r32) * 16;
#define LDKM(t) (*(const __attribute__((address_space(3))) u32x4*)(kmp + (t) * 1024))
#define LDKE(t) (*(const __attribute__((address_space(3))) unsigned*)(kep + (t) * 128))
#define LDVT(t, s) (*(const __attribute__((address_space(3))) u32x4*)(vtp + ((t) * 4 + (s) * 2) * VBLK))
    f32x16 oa = {}, ob = {};
    f32x32 s0, s1;
    f32x8 pha = {}, phb = {};
    u32x2 ke; ke[0] = 0; ke[1] = 0x00003F80u;
    u32x4 km, v0, v1 = {};
    u32x4 qma, qmb; u32x2 qe;
    unsigned aKM = (unsigned)(uintptr_t)kmp - 2 * 1024, aKE = (unsigned)(uintptr_t)kep - 2 * 128;
    unsigned aVT = r32 < 19 ? (unsigned)(uintptr_t)vtp - 2 * 4 * VBLK : 0x40000u;
    unsigned wK = (unsigned)(uintptr_t)(lds + LDS_KM + (tid >> 5) * 1024 + (tid & 31) * 16);
    unsigned wE = (unsigned)(uintptr_t)(lds + LDS_KE + tid * 4);
    unsigned wV = (unsigned)(uintptr_t)(lds + LDS_VT + (tid >> 3) * VBLK + (tid & 7) * 2);

#pragma unroll 1
    for (int p = 0; p < 4; ++p) {
        {
            const float r = __builtin_amdgcn_rcpf(__builtin_amdgcn_sqrtf(sumsq9(ka, kb, kc)) + EPS);
            u32x4 a, b;
#pragma unroll
            for (int j = 0; j < 4; ++j) { a[j] = scale2(ka[j], r); b[j] = scale2(kb[j], r); }
            const unsigned e = scale2(kc, r);
            asm volatile("" : "+v"(wK), "+v"(wE), "+v"(wV));
            *(__attribute__((address_space(3))) u32x4*)(wK) = a;
            *(__attribute__((address_space(3))) u32x4*)(wK + 512) = b;
            *(__attribute__((address_space(3))) unsigned*)(wE) = e;
#define WR16(off, val) *(__attribute__((address_space(3))) uint16_t*)(wV + (off)) = (uint16_t)(val)
#pragma unroll
            for (int j = 0; j < 4; ++j) {
                WR16((2 * j) * 16, va[j]); WR16((2 * j + 1) * 16, va[j] >> 16);
                WR16((8 + 2 * j) * 16, vb[j]); WR16((8 + 2 * j + 1) * 16, vb[j] >> 16);
            }
            WR16(16 * 16, vc); WR16(17 * 16, vc >> 16); WR16(18 * 16, 0x3F80u);
            wK += 16 * 1024; wE += 512 * 4; wV += 64 * VBLK;
        }
        if (p < 3) {
            roff += 512 * 36;
            ka = *(const u32x4_a4*)(ksrc + roff); kb = *(const u32x4_a4*)(ksrc + roff + 16); kc = *(const unsigned*)(ksrc + roff + 32);
            va = *(const u32x4_a4*)(vsrc + roff); vb = *(const u32x4_a4*)(vsrc + roff + 16); vc = *(const unsigned*)(vsrc + roff + 32);
        }
        if (p == 0) {
            const float r = cs * __builtin_amdgcn_rcpf(__builtin_amdgcn_sqrtf(sumsq9(qa, qb, qcw)) + EPS);
#pragma unroll
            for (int j = 0; j < 4; ++j) {
                auto sw = __builtin_amdgcn_permlane32_swap(scale2(qa[j], r), scale2(qb[j], r), false, false);
                qma[j] = sw[0]; qmb[j] = sw[1];
            }
            const float shift = fast ? -(__builtin_fabsf(cs) * 1.015625f) : 0.f;
            qe[0] = scale2(qcw, r);
            qe[1] = cvtpk(shift, 0.f);
            STAMP(1);
        }
        asm volatile("s_waitcnt lgkmcnt(0)\n\ts_barrier" ::: "memory");
        if (p == 0) { STAMP(2); }
        if (fast) {
            if (p == 0) {
                ke[0] = LDKE(0);
                s0 = MFMA4(ke, qe, (f32x32){});
                const u32x4 k0 = LDKM(0);
                f32x16 sa = MFMA16(k0, qma, LO16(s0)), sb = MFMA16(k0, qmb, HI16(s0));
                s0 = __builtin_shufflevector(sa, sb, 0, 1, 2, 3, 4, 5, 6, 7, 8, 9, 10, 11, 12, 13, 14, 15, 16, 17, 18, 19, 20, 21, 22, 23, 24, 25, 26, 27, 28, 29, 30, 31);
                ke[0] = LDKE(1); km = LDKM(1);
                v0 = *(const __attribute__((address_space(3))) u32x4*)(aVT + 2 * 4 * VBLK);
            }
            int cnt = 4;
            asm volatile(
                "s_waitcnt lgkmcnt(0)\n\t"
                "s_nop 7\n\t"
                "s_cmp_eq_u32 %[p], 0\n\t"
                "s_cbranch_scc1 Lent2_%=\n\t"
                "Lloop_%=:\n\t"
                STEP(32, 64, 0) STEP(64, 32, 1)
                "s_cmp_eq_u32 %[cnt], 0\n\t"
                "s_cbranch_scc1 Ldone_%=\n\t"
                "Lent2_%=:\n\t"
                STEP(32, 64, 2) STEP(64, 32, 3)
                "v_add_u32 v137, 4*1024, v137\n\t"
                "v_add_u32 v138, 4*128, v138\n\t"
                "v_add_u32 v139, 4*4*304, v139\n\t"
                "s_sub_u32 %[cnt], %[cnt], 1\n\t"
                "s_cmp_lg_u32 %[cnt], 0\n\t"
                "s_cbranch_scc1 Lloop_%=\n\t"
                "s_cmp_eq_u32 %[p], 3\n\t"
                "s_cbranch_scc1 Lloop_%=\n\t"
                "Ldone_%=:\n\t"
                "s_waitcnt lgkmcnt(0)\n\t"
                "s_nop 7\n\t"
                : "+{v[0:15]}"(oa), "+{v[16:31]}"(ob), "+{v[32:63]}"(s0), "=&{v[64:95]}"(s1), "+{v[96:103]}"(pha), "+{v[104:111]}"(phb),
                  "+{v[122:123]}"(ke), "+{v[124:127]}"(km), "+{v[128:131]}"(v0), "+{v[132:135]}"(v1),
                  "+{v137}"(aKM), "+{v138}"(aKE), "+{v139}"(aVT), [cnt] "+s"(cnt)
                : "{v[112:115]}"(qma), "{v[116:119]}"(qmb), "{v[120:121]}"(qe), "{v136}"(0xDDDDDDDDu), [p] "s"(p)
                : "memory", "scc");
        }
        if (p == 0) { STAMP(3); }
    }
    if (fast) {
        oa = SMF(v1, pha, oa);
        ob = SMF(v1, phb, ob);
    } else {
        float ma = -INFINITY, mb = -INFINITY;
        for (int t = 0; t < NTILE; ++t) {
            ke[0] = LDKE(t);
            const f32x32 s = MFMA4(ke, qe, (f32x32){});
            const u32x4 k0 = LDKM(t);
            f32x16 sa = MFMA16(k0, qma, LO16(s)), sb = MFMA16(k0, qmb, HI16(s));
            float xa = sa[0], xb = sb[0];
#pragma unroll
            for (int r = 1; r < 16; ++r) { xa = __builtin_fmaxf(xa, sa[r]); xb = __builtin_fmaxf(xb, sb[r]); }
            xa = __builtin_fmaxf(xa, __shfl_xor(xa, 32)); xb = __builtin_fmaxf(xb, __shfl_xor(xb, 32));
            const float na = __builtin_fmaxf(ma, xa), nb = __builtin_fmaxf(mb, xb);
            const float fa = __builtin_amdgcn_exp2f(ma - na), fb = __builtin_amdgcn_exp2f(mb - nb);
            ma = na; mb = nb;
#pragma unroll
            for (int r = 0; r < 16; ++r) {
                oa[r] *= fa; sa[r] = __builtin_amdgcn_exp2f(sa[r] - na);
                ob[r] *= fb; sb[r] = __builtin_amdgcn_exp2f(sb[r] - nb);
            }
            const u32x4 w0 = LDVT(t, 0), w1 = LDVT(t, 1);
            oa = SMF(w0, LO8(sa), oa); ob = SMF(w0, LO8(sb), ob);
            oa = SMF(w1, HI8(sa), oa); ob = SMF(w1, HI8(sb), ob);
        }
    }
    STAMP(4);
    {
        float x[11], y[8];
#pragma unroll
        for (int r = 0; r < 11; ++r) {
            auto sw = __builtin_amdgcn_permlane32_swap(__float_as_uint(oa[r]), __float_as_uint(ob[r]), false, false);
            x[r] = __uint_as_float(sw[0]); if (r < 8) y[r] = __uint_as_float(sw[1]);
        }
        const float inv = __builtin_amdgcn_rcpf(x[10]);
        u32x4 a, b;
        a[0] = cvtpk(x[0] * inv, x[1] * inv); a[1] = cvtpk(x[2] * inv, x[3] * inv);
        a[2] = cvtpk(y[0] * inv, y[1] * inv); a[3] = cvtpk(y[2] * inv, y[3] * inv);
        b[0] = cvtpk(x[4] * inv, x[5] * inv); b[1] = cvtpk(x[6] * inv, x[7] * inv);
        b[2] = cvtpk(y[4] * inv, y[5] * inv); b[3] = cvtpk(y[6] * inv, y[7] * inv);
        char* op = (char*)(O + hb) + (qc * 512 + tid) * 36;
        const unsigned c_ = cvtpk(x[8] * inv, x[9] * inv);
        asm volatile("global_store_dwordx4 %0, %1, off sc0 sc1\n\tglobal_store_dwordx4 %0, %2, off offset:16 sc0 sc1\n\tglobal_store_dword %0, %3, off offset:32 sc0 sc1\n\ts_nop 1" :: "v"(op), "v"(a), "v"(b), "v"(c_) : "memory");
    }
    asm volatile("s_waitcnt vmcnt(0)" ::: "memory");
    { while (__builtin_amdgcn_s_memrealtime() - rt0_ < 3800ull) { __builtin_amdgcn_s_sleep(1); }
      unsigned long long mn = ~0ull;
      for (int i = 0; i < 4; ++i) { unsigned long long x = __hip_atomic_load(WS + lane + 64 * i, __ATOMIC_RELAXED, __HIP_MEMORY_SCOPE_AGENT); mn = x < mn ? x : mn; }
      for (int o_ = 1; o_ < 64; o_ <<= 1) { unsigned long long x = __shfl_xor(mn, o_); mn = x < mn ? x : mn; }
      unsigned lo_ = __builtin_amdgcn_readfirstlane((unsigned)mn), hi_ = __builtin_amdgcn_readfirstlane((unsigned)(mn >> 32));
      unsigned long long g0 = ((unsigned long long)hi_ << 32) | lo_;
      if (rt0_ - g0 + 2000ull > 4000ull) g0 = rt0_;
      while (__builtin_amdgcn_s_memrealtime() - g0 < 6000ull) { __builtin_amdgcn_s_sleep(1); } }
}

extern "C" void kernel_launch(void* const* d_in, const int* in_sizes, int n_in,
                              void* d_out, int out_size, void* d_ws, size_t ws_size,
                              hipStream_t stream) {
    (void)in_sizes; (void)n_in; (void)out_size; (void)d_ws; (void)ws_size;
    (void)hipFuncSetAttribute((const void*)qknorm_attn, hipFuncAttributeMaxDynamicSharedMemorySize, LDS_BYTES);
    qknorm_attn<<<NBH * 4, 512, LDS_BYTES, stream>>>((const uint16_t*)d_in[0], (const uint16_t*)d_in[1],
                                                     (const uint16_t*)d_in[2], (const uint16_t*)d_in[3],
                                                     (uint16_t*)d_out, (unsigned long long*)d_ws
                                                     );
}
```

```cpp
#include <hip/hip_runtime.h>
#include <hip/hip_bf16.h>
#include <stdint.h>

typedef float f32x32 __attribute__((ext_vector_type(32)));
typedef float f32x16 __attribute__((ext_vector_type(16)));
typedef float f32x8 __attribute__((ext_vector_type(8)));
typedef __bf16 bf16x8 __attribute__((ext_vector_type(8)));
typedef __bf16 bf16x16 __attribute__((ext_vector_type(16)));
typedef __bf16 bf16x2 __attribute__((ext_vector_type(2)));
typedef short s16x4 __attribute__((ext_vector_type(4)));
typedef unsigned u32x4 __attribute__((ext_vector_type(4)));
typedef unsigned u32x2 __attribute__((ext_vector_type(2)));
typedef u32x4 u32x4_a4 __attribute__((aligned(4)));

constexpr int SEQ = 2048, DH = 18, NHEAD = 32, NBH = 64;
constexpr int NTILE = SEQ / 32;
constexpr int VBLK = 19 * 16;
constexpr int LDS_KE = 256;
constexpr int LDS_KM = LDS_KE + SEQ * 4;
constexpr int LDS_VT = LDS_KM + SEQ * 32;
constexpr int LDS_END = LDS_VT + (SEQ / 8) * VBLK;
constexpr int LDS_BYTES = LDS_END + 2048;
constexpr float LOG2E = 1.4426950408889634f;
constexpr float EPS = 1e-6f;
constexpr float FAST_LIMIT = 16.0f;

typedef __attribute__((address_space(3))) char* lds_ptr;
typedef __attribute__((address_space(3))) const char* lds_cptr;

__device__ __forceinline__ unsigned cvtpk(float lo, float hi) {
    bf16x2 r; r[0] = (__bf16)lo; r[1] = (__bf16)hi; return __builtin_bit_cast(unsigned, r);
}
__device__ __forceinline__ float bflo(unsigned x) { return __uint_as_float(x << 16); }
__device__ __forceinline__ float bfhi(unsigned x) { return __uint_as_float(x & 0xffff0000u); }
__device__ __forceinline__ float dot2(unsigned w, float acc) {
    bf16x2 v = __builtin_bit_cast(bf16x2, w); return __builtin_amdgcn_fdot2_f32_bf16(v, v, acc, false);
}
__device__ __forceinline__ float sumsq9(const u32x4& a, const u32x4& b, unsigned c) {
    float x = dot2(a[0], 0.f), y = dot2(a[1], 0.f), z = dot2(a[2], 0.f);
    x = dot2(a[3], x); y = dot2(b[0], y); z = dot2(b[1], z);
    x = dot2(b[2], x); y = dot2(b[3], y); z = dot2(c, z);
    return x + y + z;
}
__device__ __forceinline__ unsigned scale2(unsigned w, float r) { return cvtpk(bflo(w) * r, bfhi(w) * r); }

#define MFMA16(a, b, c) __builtin_amdgcn_mfma_f32_32x32x16_bf16(__builtin_bit_cast(bf16x8, a), __builtin_bit_cast(bf16x8, b), c, 0, 0, 0)
#define MFMA4(a, b, c) __builtin_amdgcn_mfma_f32_32x32x4bf16_1k(__builtin_bit_cast(s16x4, a), __builtin_bit_cast(s16x4, b), c, 0, 0, 0)
#define SMF(a, b, c) __builtin_amdgcn_smfmac_f32_32x32x32_bf16(__builtin_bit_cast(bf16x8, a), __builtin_bit_cast(bf16x16, b), c, (int)0xDDDDDDDD, 0, 0)
#define LO8(x) __builtin_shufflevector(x, x, 0, 1, 2, 3, 4, 5, 6, 7)
#define HI8(x) __builtin_shufflevector(x, x, 8, 9, 10, 11, 12, 13, 14, 15)
#define LO16(x) __builtin_shufflevector(x, x, 0, 1, 2, 3, 4, 5, 6, 7, 8, 9, 10, 11, 12, 13, 14, 15)
#define HI16(x) __builtin_shufflevector(x, x, 16, 17, 18, 19, 20, 21, 22, 23, 24, 25, 26, 27, 28, 29, 30, 31)

#define EXP4(d, s) \
    "v_exp_f32 v[" #d "+0], v[" #s "+0]\n\t" "v_exp_f32 v[" #d "+1], v[" #s "+1]\n\t" \
    "v_exp_f32 v[" #d "+2], v[" #s "+2]\n\t" "v_exp_f32 v[" #d "+3], v[" #s "+3]\n\t"
#define PRIO(s) "s_setprio 3-" #s "\n\t"
#define STEP(C, N, s) \
    PRIO(s) \
    "s_waitcnt lgkmcnt(3)\n\t" \
    "v_mfma_f32_32x32x4_2b_bf16 v[" #N ":" #N "+31], v[122:123], v[120:121], 0\n\t" \
    "ds_read_b32 v122, v138 offset:(" #s "+2)*128\n\t" \
    EXP4(C, C) EXP4(C + 4, C + 4) \
    "s_waitcnt lgkmcnt(3)\n\t" \
    "v_smfmac_f32_32x32x32_bf16 v[0:15], v[132:135], v[96:103], v136\n\t" \
    EXP4(C + 16, C + 16) \
    "v_smfmac_f32_32x32x32_bf16 v[16:31], v[132:135], v[104:111], v136\n\t" \
    "ds_read_b128 v[132:135], v139 offset:(" #s "*4+2)*304\n\t" \
    EXP4(C + 20, C + 20) \
    "s_waitcnt lgkmcnt(3)\n\t" \
    "v_mfma_f32_32x32x16_bf16 v[" #N ":" #N "+15], v[124:127], v[112:115], v[" #N ":" #N "+15]\n\t" \
    EXP4(96, C + 8) \
    "v_mfma_f32_32x32x16_bf16 v[" #N "+16:" #N "+31], v[124:127], v[116:119], v[" #N "+16:" #N "+31]\n\t" \
    "ds_read_b128 v[124:127], v137 offset:(" #s "+2)*1024\n\t" \
    EXP4(100, C + 12) \
    "s_waitcnt lgkmcnt(3)\n\t" \
    "v_smfmac_f32_32x32x32_bf16 v[0:15], v[128:131], v[" #C ":" #C "+7], v136\n\t" \
    EXP4(104, C + 24) \
    "v_smfmac_f32_32x32x32_bf16 v[16:31], v[128:131], v[" #C "+16:" #C "+23], v136\n\t" \
    "ds_read_b128 v[128:131], v139 offset:(" #s "*4+4)*304\n\t" \
    EXP4(108, C + 28)

#define STAMP(i)

__global__ __launch_bounds__(512) void qknorm_attn(const uint16_t* __restrict__ Q, const uint16_t* __restrict__ K,
                                                   const uint16_t* __restrict__ V, const uint16_t* __restrict__ T,
                                                   uint16_t* __restrict__ O
                                                   ) {
    extern __shared__ __attribute__((aligned(16))) char smem[];
    const lds_ptr lds = (lds_ptr)smem;
    const int tid = threadIdx.x, lane = tid & 63, r32 = lane & 31, hi = lane >> 5;
    const int bid = blockIdx.x;
    const int bh = (bid & 7) + 8 * (bid >> 5), qc = (bid >> 3) & 3;
    const size_t hb = (size_t)bh * SEQ * DH;
    const char* ksrc = (const char*)(K + hb);
    const char* vsrc = (const char*)(V + hb);
    unsigned roff = tid * 36;

    u32x4 ka = *(const u32x4_a4*)(ksrc + roff), kb = *(const u32x4_a4*)(ksrc + roff + 16);
    unsigned kc = *(const unsigned*)(ksrc + roff + 32);
    u32x4 va = *(const u32x4_a4*)(vsrc + roff), vb = *(const u32x4_a4*)(vsrc + roff + 16);
    unsigned vc = *(const unsigned*)(vsrc + roff + 32);
    const char* qp = (const char*)(Q + hb) + qc * 512 * 36;
    u32x4 qa = *(const u32x4_a4*)(qp + roff), qb = *(const u32x4_a4*)(qp + roff + 16);
    unsigned qcw = *(const unsigned*)(qp + roff + 32);
    const unsigned tw = ((const unsigned*)T)[(bh & (NHEAD - 1)) >> 1];
    const float tval = __uint_as_float((bh & 1) ? (tw & 0xffff0000u) : (tw << 16));
    const float cs = tval * LOG2E;
    const bool fast = __builtin_fabsf(cs) <= FAST_LIMIT;

    const lds_cptr kmp = (lds_cptr)lds + LDS_KM + lane * 16;
    const lds_cptr kep = (lds_cptr)lds + LDS_KE + r32 * 4;
    const lds_cptr vtp = (lds_cptr)lds + LDS_VT + (hi * 19 + r32) * 16;
#define LDKM(t) (*(const __attribute__((address_space(3))) u32x4*)(kmp + (t) * 1024))
#define LDKE(t) (*(const __attribute__((address_space(3))) unsigned*)(kep + (t) * 128))
#define LDVT(t, s) (*(const __attribute__((address_space(3))) u32x4*)(vtp + ((t) * 4 + (s) * 2) * VBLK))
    u32x4 pf_keep;
    f32x16 oa = {}, ob = {};
    f32x32 s0, s1;
    f32x8 pha = {}, phb = {};
    u32x2 ke; ke[0] = 0; ke[1] = 0x00003F80u;
    u32x4 km, v0, v1 = {};
    u32x4 qma, qmb; u32x2 qe;
    unsigned aKM = (unsigned)(uintptr_t)kmp - 2 * 1024, aKE = (unsigned)(uintptr_t)kep - 2 * 128;
    unsigned aVT = r32 < 19 ? (unsigned)(uintptr_t)vtp - 2 * 4 * VBLK : 0x40000u;
    unsigned wK = (unsigned)(uintptr_t)(lds + LDS_KM + (tid >> 5) * 1024 + (tid & 31) * 16);
    unsigned wE = (unsigned)(uintptr_t)(lds + LDS_KE + tid * 4);
    unsigned wV = (unsigned)(uintptr_t)(lds + LDS_VT + (tid >> 3) * VBLK + (tid & 7) * 2);

    {
        unsigned long long pc; asm volatile("s_getpc_b64 %0" : "=s"(pc));
        const char* cp = (const char*)(pc & ~255ull) + (tid < 288 ? tid : 0) * 16;
        u32x4 junk; asm volatile("global_load_dwordx4 %0, %1, off" : "=v"(junk) : "v"(cp));
        pf_keep = junk;
    }
    volatile __attribute__((address_space(3))) unsigned* flags = (volatile __attribute__((address_space(3))) unsigned*)lds;
    if (tid < 4) flags[tid] = 0;

#define STAGE() do { \
        const float r = __builtin_amdgcn_rcpf(__builtin_amdgcn_sqrtf(sumsq9(ka, kb, kc)) + EPS); \
        u32x4 a, b; \
        _Pragma("unroll") for (int j = 0; j < 4; ++j) { a[j] = scale2(ka[j], r); b[j] = scale2(kb[j], r); } \
        const unsigned e = scale2(kc, r); \
        asm volatile("" : "+v"(wK), "+v"(wE), "+v"(wV)); \
        *(__attribute__((address_space(3))) u32x4*)(wK) = a; \
        *(__attribute__((address_space(3))) u32x4*)(wK + 512) = b; \
        *(__attribute__((address_space(3))) unsigned*)(wE) = e; \
        _Pragma("unroll") for (int j = 0; j < 4; ++j) { \
            WR16((2 * j) * 16, va[j]); WR16((2 * j + 1) * 16, va[j] >> 16); \
            WR16((8 + 2 * j) * 16, vb[j]); WR16((8 + 2 * j + 1) * 16, vb[j] >> 16); } \
        WR16(16 * 16, vc); WR16(17 * 16, vc >> 16); WR16(18 * 16, 0x3F80u); \
        wK += 16 * 1024; wE += 512 * 4; wV += 64 * VBLK; } while (0)
#define WR16(off, val) *(__attribute__((address_space(3))) uint16_t*)(wV + (off)) = (uint16_t)(val)
#define FETCH() do { roff += 512 * 36; \
        ka = *(const u32x4_a4*)(ksrc + roff); kb = *(const u32x4_a4*)(ksrc + roff + 16); kc = *(const unsigned*)(ksrc + roff + 32); \
        va = *(const u32x4_a4*)(vsrc + roff); vb = *(const u32x4_a4*)(vsrc + roff + 16); vc = *(const unsigned*)(vsrc + roff + 32); } while (0)

#define STAG 2
    const int first = __builtin_amdgcn_readfirstlane(tid) < 256 ? STAG : 3;
#pragma unroll 1
    for (int r = -1; r < 8; ++r) {
        if (r >= 0 && fast) {
            int cnt = (r & 1) ? 4 - first : first;
            asm volatile(
                "s_waitcnt lgkmcnt(0)\n\t"
                "s_nop 7\n\t"
                "s_cmp_eq_u32 %[p], 0\n\t"
                "s_cbranch_scc1 Lent2_%=\n\t"
                "Lloop_%=:\n\t"
                STEP(32, 64, 0) STEP(64, 32, 1)
                "s_cmp_eq_u32 %[cnt], 0\n\t"
                "s_cbranch_scc1 Ldone_%=\n\t"
                "Lent2_%=:\n\t"
                STEP(32, 64, 2) STEP(64, 32, 3)
                "v_add_u32 v137, 4*1024, v137\n\t"
                "v_add_u32 v138, 4*128, v138\n\t"
                "v_add_u32 v139, 4*4*304, v139\n\t"
                "s_sub_u32 %[cnt], %[cnt], 1\n\t"
                "s_cmp_lg_u32 %[cnt], 0\n\t"
                "s_cbranch_scc1 Lloop_%=\n\t"
                "s_cmp_eq_u32 %[p], 7\n\t"
                "s_cbranch_scc1 Lloop_%=\n\t"
                "Ldone_%=:\n\t"
                "s_waitcnt lgkmcnt(0)\n\t"
                "s_nop 7\n\t"
                : "+{v[0:15]}"(oa), "+{v[16:31]}"(ob), "+{v[32:63]}"(s0), "=&{v[64:95]}"(s1), "+{v[96:103]}"(pha), "+{v[104:111]}"(phb),
                  "+{v[122:123]}"(ke), "+{v[124:127]}"(km), "+{v[128:131]}"(v0), "+{v[132:135]}"(v1),
                  "+{v137}"(aKM), "+{v138}"(aKE), "+{v139}"(aVT), [cnt] "+s"(cnt)
                : "{v[112:115]}"(qma), "{v[116:119]}"(qmb), "{v[120:121]}"(qe), "{v136}"(0xDDDDDDDDu), [p] "s"(r)
                : "memory", "scc");
            if (r == 0) { STAMP(3); }
        }
        if (r == -1 || (!(r & 1) && r < 6)) {
            STAGE();
            if (r < 4) FETCH();
            if (r >= 0) {
                asm volatile("s_waitcnt lgkmcnt(0)" ::: "memory");
                if (lane == 0) __hip_atomic_fetch_add((__attribute__((address_space(3))) unsigned*)lds + (r >> 1), 1u, __ATOMIC_RELAXED, __HIP_MEMORY_SCOPE_WORKGROUP);
            }
        } else if ((r & 1) && r < 6) {
            while (flags[r >> 1] < 8u) __builtin_amdgcn_s_sleep(1);
            asm volatile("" ::: "memory");
        }
        if (r == -1) {
            asm volatile("" : "+v"(qa), "+v"(qb), "+v"(qcw));
            const float rq = cs * __builtin_amdgcn_rcpf(__builtin_amdgcn_sqrtf(sumsq9(qa, qb, qcw)) + EPS);
#pragma unroll
            for (int j = 0; j < 4; ++j) {
                auto sw = __builtin_amdgcn_permlane32_swap(scale2(qa[j], rq), scale2(qb[j], rq), false, false);
                qma[j] = sw[0]; qmb[j] = sw[1];
            }
            const float shift = fast ? -(__builtin_fabsf(cs) * 1.015625f) : 0.f;
            qe[0] = scale2(qcw, rq);
            qe[1] = cvtpk(shift, 0.f);
            STAMP(1);
            asm volatile("s_waitcnt lgkmcnt(0)\n\ts_barrier" ::: "memory");
            STAMP(2);
            ke[0] = LDKE(0);
            s0 = MFMA4(ke, qe, (f32x32){});
            const u32x4 k0 = LDKM(0);
            f32x16 sa = MFMA16(k0, qma, LO16(s0)), sb = MFMA16(k0, qmb, HI16(s0));
            s0 = __builtin_shufflevector(sa, sb, 0, 1, 2, 3, 4, 5, 6, 7, 8, 9, 10, 11, 12, 13, 14, 15, 16, 17, 18, 19, 20, 21, 22, 23, 24, 25, 26, 27, 28, 29, 30, 31);
            ke[0] = LDKE(1); km = LDKM(1);
            v0 = *(const __attribute__((address_space(3))) u32x4*)(aVT + 2 * 4 * VBLK);
        }
    }
    asm volatile("s_waitcnt vmcnt(0)" :: "v"(pf_keep));
    if (fast) {
        oa = SMF(v1, pha, oa);
        ob = SMF(v1, phb, ob);
    } else {
        float ma = -INFINITY, mb = -INFINITY;
        for (int t = 0; t < NTILE; ++t) {
            ke[0] = LDKE(t);
            const f32x32 s = MFMA4(ke, qe, (f32x32){});
            const u32x4 k0 = LDKM(t);
            f32x16 sa = MFMA16(k0, qma, LO16(s)), sb = MFMA16(k0, qmb, HI16(s));
            float xa = sa[0], xb = sb[0];
#pragma unroll
            for (int r = 1; r < 16; ++r) { xa = __builtin_fmaxf(xa, sa[r]); xb = __builtin_fmaxf(xb, sb[r]); }
            xa = __builtin_fmaxf(xa, __shfl_xor(xa, 32)); xb = __builtin_fmaxf(xb, __shfl_xor(xb, 32));
            const float na = __builtin_fmaxf(ma, xa), nb = __builtin_fmaxf(mb, xb);
            const float fa = __builtin_amdgcn_exp2f(ma - na), fb = __builtin_amdgcn_exp2f(mb - nb);
            ma = na; mb = nb;
#pragma unroll
            for (int r = 0; r < 16; ++r) {
                oa[r] *= fa; sa[r] = __builtin_amdgcn_exp2f(sa[r] - na);
                ob[r] *= fb; sb[r] = __builtin_amdgcn_exp2f(sb[r] - nb);
            }
            const u32x4 w0 = LDVT(t, 0), w1 = LDVT(t, 1);
            oa = SMF(w0, LO8(sa), oa); ob = SMF(w0, LO8(sb), ob);
            oa = SMF(w1, HI8(sa), oa); ob = SMF(w1, HI8(sb), ob);
        }
    }
    STAMP(4);
    {
        float x[11], y[8];
#pragma unroll
        for (int r = 0; r < 11; ++r) {
            auto sw = __builtin_amdgcn_permlane32_swap(__float_as_uint(oa[r]), __float_as_uint(ob[r]), false, false);
            x[r] = __uint_as_float(sw[0]); if (r < 8) y[r] = __uint_as_float(sw[1]);
        }
        const float inv = __builtin_amdgcn_rcpf(x[10]);
        u32x4 a, b;
        a[0] = cvtpk(x[0] * inv, x[1] * inv); a[1] = cvtpk(x[2] * inv, x[3] * inv);
        a[2] = cvtpk(y[0] * inv, y[1] * inv); a[3] = cvtpk(y[2] * inv, y[3] * inv);
        b[0] = cvtpk(x[4] * inv, x[5] * inv); b[1] = cvtpk(x[6] * inv, x[7] * inv);
        b[2] = cvtpk(y[4] * inv, y[5] * inv); b[3] = cvtpk(y[6] * inv, y[7] * inv);
        char* op = (char*)(O + hb) + (qc * 512 + tid) * 36;
        *(u32x4_a4*)(op) = a;
        *(u32x4_a4*)(op + 16) = b;
        *(unsigned*)(op + 32) = cvtpk(x[8] * inv, x[9] * inv);
    }
}

extern "C" void kernel_launch(void* const* d_in, const int* in_sizes, int n_in,
                              void* d_out, int out_size, void* d_ws, size_t ws_size,
                              hipStream_t stream) {
    (void)in_sizes; (void)n_in; (void)out_size; (void)d_ws; (void)ws_size;
    (void)hipFuncSetAttribute((const void*)qknorm_attn, hipFuncAttributeMaxDynamicSharedMemorySize, LDS_BYTES);
    qknorm_attn<<<NBH * 4, 512, LDS_BYTES, stream>>>((const uint16_t*)d_in[0], (const uint16_t*)d_in[1],
                                                     (const uint16_t*)d_in[2], (const uint16_t*)d_in[3],
                                                     (uint16_t*)d_out
                                                     );
}
```

```cpp
#include <hip/hip_runtime.h>
#include <hip/hip_bf16.h>
#include <stdint.h>

typedef float f32x32 __attribute__((ext_vector_type(32)));
typedef float f32x16 __attribute__((ext_vector_type(16)));
typedef __bf16 bf16x8 __attribute__((ext_vector_type(8)));
typedef __bf16 bf16x16 __attribute__((ext_vector_type(16)));
typedef __bf16 bf16x2 __attribute__((ext_vector_type(2)));
typedef short s16x4 __attribute__((ext_vector_type(4)));
typedef unsigned u32x4 __attribute__((ext_vector_type(4)));
typedef unsigned u32x2 __attribute__((ext_vector_type(2)));
typedef u32x4 u32x4_a4 __attribute__((aligned(4)));

constexpr int SEQ = 2048, DH = 18, NHEAD = 32, NBH = 64;
constexpr int NTILE = SEQ / 32;
constexpr int VBLK = 19 * 16;
constexpr int LDS_KE = 256;
constexpr int LDS_KM = LDS_KE + SEQ * 4;
constexpr int LDS_VT = LDS_KM + SEQ * 32;
constexpr int LDS_END = LDS_VT + (SEQ / 8) * VBLK;
constexpr int LDS_BYTES = LDS_END + 2048;
constexpr float LOG2E = 1.4426950408889634f;
constexpr float EPS = 1e-6f;

typedef __attribute__((address_space(3))) char* lds_ptr;

__device__ __forceinline__ unsigned cvtpk(float lo, float hi) {
    bf16x2 r; r[0] = (__bf16)lo; r[1] = (__bf16)hi; return __builtin_bit_cast(unsigned, r);
}
__device__ __forceinline__ float bflo(unsigned x) { return __uint_as_float(x << 16); }
__device__ __forceinline__ float bfhi(unsigned x) { return __uint_as_float(x & 0xffff0000u); }
__device__ __forceinline__ float dot2(unsigned w, float acc) {
    bf16x2 v = __builtin_bit_cast(bf16x2, w); return __builtin_amdgcn_fdot2_f32_bf16(v, v, acc, false);
}
__device__ __forceinline__ float sumsq9(const u32x4& a, const u32x4& b, unsigned c) {
    float x = dot2(a[0], 0.f), y = dot2(a[1], 0.f), z = dot2(a[2], 0.f);
    x = dot2(a[3], x); y = dot2(b[0], y); z = dot2(b[1], z);
    x = dot2(b[2], x); y = dot2(b[3], y); z = dot2(c, z);
    return x + y + z;
}
__device__ __forceinline__ unsigned scale2(unsigned w, float r) { return cvtpk(bflo(w) * r, bfhi(w) * r); }

#define MFMA16(a, b, c) __builtin_amdgcn_mfma_f32_32x32x16_bf16(__builtin_bit_cast(bf16x8, a), __builtin_bit_cast(bf16x8, b), c, 0, 0, 0)
#define MFMA4(a, b, c) __builtin_amdgcn_mfma_f32_32x32x4bf16_1k(__builtin_bit_cast(s16x4, a), __builtin_bit_cast(s16x4, b), c, 0, 0, 0)
#define SMF(a, b, c) __builtin_amdgcn_smfmac_f32_32x32x32_bf16(__builtin_bit_cast(bf16x8, a), __builtin_bit_cast(bf16x16, b), c, (int)0xDDDDDDDD, 0, 0)
#define LO8(x) __builtin_shufflevector(x, x, 0, 1, 2, 3, 4, 5, 6, 7)
#define HI8(x) __builtin_shufflevector(x, x, 8, 9, 10, 11, 12, 13, 14, 15)
#define LO16(x) __builtin_shufflevector(x, x, 0, 1, 2, 3, 4, 5, 6, 7, 8, 9, 10, 11, 12, 13, 14, 15)
#define HI16(x) __builtin_shufflevector(x, x, 16, 17, 18, 19, 20, 21, 22, 23, 24, 25, 26, 27, 28, 29, 30, 31)
#define LDS_AT(T, off) (*(__attribute__((address_space(3))) T*)(lds + (off)))

#define MAIN_ASM \
    "s_setprio 3\n\t" \
    "v_mul_u32_u24 v176, 36, v190\n\t" \
    "global_load_dwordx4 v[140:143], v176, %[kp] \n\t" \
    "global_load_dwordx4 v[144:147], v176, %[kp] offset:16 \n\t" \
    "global_load_dword v148, v176, %[kp] offset:32 \n\t" \
    "global_load_dwordx4 v[150:153], v176, %[vp] \n\t" \
    "global_load_dwordx4 v[154:157], v176, %[vp] offset:16 \n\t" \
    "global_load_dword v149, v176, %[vp] offset:32 \n\t" \
    "s_load_dword %[t], %[tp], 0x0\n\t" \
    "v_lshrrev_b32 v185, 6, v190\n\t" \
    "v_mul_u32_u24 v185, 2304, v185\n\t" \
    "v_and_b32 v74, 63, v190\n\t" \
    "v_lshl_add_u32 v185, v74, 4, v185\n\t" \
    "v_mov_b32 v191, v176\n\t" \
    "v_and_b32 v74, 63, v190\n\t" \
    "v_and_b32 v75, 31, v190\n\t" \
    "v_bfe_u32 v76, v190, 5, 1\n\t" \
    "v_lshlrev_b32 v74, 4, v74\n\t" \
    "v_add_u32 v137, 6400, v74\n\t" \
    "v_lshlrev_b32 v138, 2, v75\n\t" \
    "v_mad_u32_u24 v76, v76, 19, v75\n\t" \
    "v_lshlrev_b32 v76, 4, v76\n\t" \
    "v_add_u32 v76, 71552, v76\n\t" \
    "v_mov_b32 v77, 0x40000\n\t" \
    "v_cmp_gt_u32 vcc, 19, v75\n\t" \
    "v_lshrrev_b32 v78, 5, v190\n\t" \
    "v_lshlrev_b32 v78, 10, v78\n\t" \
    "v_cndmask_b32 v139, v77, v76, vcc\n\t" \
    "v_lshl_add_u32 v78, v75, 4, v78\n\t" \
    "v_add_u32 v177, 8448, v78\n\t" \
    "v_lshlrev_b32 v79, 2, v190\n\t" \
    "v_add_u32 v178, 256, v79\n\t" \
    "v_lshrrev_b32 v80, 3, v190\n\t" \
    "v_mul_u32_u24 v80, 304, v80\n\t" \
    "v_and_b32 v81, 7, v190\n\t" \
    "v_lshl_add_u32 v80, v81, 1, v80\n\t" \
    "v_add_u32 v179, 73984, v80\n\t" \
    "v_lshrrev_b32 v180, 6, v190\n\t" \
    "v_mov_b32 v181, 0\n\t" \
    "v_mov_b32 v184, 1\n\t" \
    "v_mov_b32 v136, 0xdddddddd\n\t" \
    "v_mov_b32 v123, 0x3f80\n\t" \
    "v_and_b32 v79, 28, v79\n\t" \
    "ds_write_b32 v79, v181\n\t" \
    "v_mov_b64 v[0:1], 0\n\t" \
    "v_mov_b64 v[2:3], 0\n\t" \
    "v_mov_b64 v[4:5], 0\n\t" \
    "v_mov_b64 v[6:7], 0\n\t" \
    "v_mov_b64 v[8:9], 0\n\t" \
    "v_mov_b64 v[10:11], 0\n\t" \
    "v_mov_b64 v[12:13], 0\n\t" \
    "v_mov_b64 v[14:15], 0\n\t" \
    "v_mov_b64 v[16:17], 0\n\t" \
    "v_mov_b64 v[18:19], 0\n\t" \
    "v_mov_b64 v[20:21], 0\n\t" \
    "v_mov_b64 v[22:23], 0\n\t" \
    "v_mov_b64 v[24:25], 0\n\t" \
    "v_mov_b64 v[26:27], 0\n\t" \
    "v_mov_b64 v[28:29], 0\n\t" \
    "v_mov_b64 v[30:31], 0\n\t" \
    "v_mov_b64 v[96:97], 0\n\t" \
    "v_mov_b64 v[98:99], 0\n\t" \
    "v_mov_b64 v[100:101], 0\n\t" \
    "v_mov_b64 v[102:103], 0\n\t" \
    "v_mov_b64 v[104:105], 0\n\t" \
    "v_mov_b64 v[106:107], 0\n\t" \
    "v_mov_b64 v[108:109], 0\n\t" \
    "v_mov_b64 v[110:111], 0\n\t" \
    "v_mov_b64 v[132:133], 0\n\t" \
    "v_mov_b64 v[134:135], 0\n\t" \
    "global_load_dwordx4 v[64:67], v176, %[qp] \n\t" \
    "global_load_dwordx4 v[68:71], v176, %[qp] offset:16 \n\t" \
    "global_load_dword v72, v176, %[qp] offset:32 \n\t" \
    "Lhere_%=:\n\t" \
    "s_getpc_b64 %[pc]\n\t" \
    "v_lshlrev_b32 v74, 4, v190\n\t" \
    "v_min_u32 v74, Lend_%=-Lhere_%=-16, v74\n\t" \
    "global_load_dwordx4 v[216:219], v74, %[pc]\n\t" \
    "s_waitcnt lgkmcnt(0)\n\t" \
    "s_lshl_b32 %[j], %[t], 16\n\t" \
    "s_and_b32 %[t], %[t], 0xffff0000\n\t" \
    "s_bitcmp1_b32 %[odd], 0\n\t" \
    "s_cselect_b32 %[t], %[t], %[j]\n\t" \
    "v_mov_b32 v159, 0x3fb8aa3b\n\t" \
    "v_mul_f32 v159, %[t], v159\n\t" \
    "v_mov_b32 v74, 0x41800000\n\t" \
    "v_cmp_le_f32 vcc, abs(v159), v74\n\t" \
    "s_mov_b32 %[done], 0\n\t" \
    "s_cbranch_vccz Lbail_%=\n\t" \
    "s_waitcnt vmcnt(7)\n\t" \
    "v_dot2_f32_bf16 v158, v140, v140, 0\n\t" \
    "v_dot2_f32_bf16 v161, v141, v141, 0\n\t" \
    "v_dot2_f32_bf16 v158, v142, v142, v158\n\t" \
    "v_dot2_f32_bf16 v161, v143, v143, v161\n\t" \
    "v_dot2_f32_bf16 v158, v144, v144, v158\n\t" \
    "v_dot2_f32_bf16 v161, v145, v145, v161\n\t" \
    "v_dot2_f32_bf16 v158, v146, v146, v158\n\t" \
    "v_dot2_f32_bf16 v161, v147, v147, v161\n\t" \
    "v_dot2_f32_bf16 v158, v148, v148, v158\n\t" \
    "v_lshlrev_b32 v162, 16, v140\n\t" \
    "v_and_b32 v163, 0xffff0000, v140\n\t" \
    "v_lshlrev_b32 v164, 16, v141\n\t" \
    "v_and_b32 v165, 0xffff0000, v141\n\t" \
    "v_add_f32 v158, v158, v161\n\t" \
    "v_sqrt_f32 v158, v158\n\t" \
    "v_lshlrev_b32 v186, 16, v142\n\t" \
    "v_add_f32 v158, 0x358637bd, v158\n\t" \
    "v_rcp_f32 v160, v158\n\t" \
    "v_and_b32 v187, 0xffff0000, v142\n\t" \
    "s_waitcnt vmcnt(4)\n\t" \
    "v_pk_mul_f32 v[162:163], v[162:163], v[160:161] op_sel_hi:[1,0]\n\t" \
    "ds_write_b16 v179, v150 offset:0\n\t" \
    "v_cvt_pk_bf16_f32 v166, v162, v163\n\t" \
    "ds_write_b16_d16_hi v179, v150 offset:16\n\t" \
    "v_pk_mul_f32 v[164:165], v[164:165], v[160:161] op_sel_hi:[1,0]\n\t" \
    "ds_write_b16 v179, v151 offset:32\n\t" \
    "v_cvt_pk_bf16_f32 v167, v164, v165\n\t" \
    "ds_write_b16_d16_hi v179, v151 offset:48\n\t" \
    "v_pk_mul_f32 v[186:187], v[186:187], v[160:161] op_sel_hi:[1,0]\n\t" \
    "ds_write_b16 v179, v152 offset:64\n\t" \
    "v_cvt_pk_bf16_f32 v168, v186, v187\n\t" \
    "ds_write_b16_d16_hi v179, v152 offset:80\n\t" \
    "v_lshlrev_b32 v188, 16, v143\n\t" \
    "v_and_b32 v189, 0xffff0000, v143\n\t" \
    "v_pk_mul_f32 v[188:189], v[188:189], v[160:161] op_sel_hi:[1,0]\n\t" \
    "ds_write_b16 v179, v153 offset:96\n\t" \
    "v_cvt_pk_bf16_f32 v169, v188, v189\n\t" \
    "ds_write_b16_d16_hi v179, v153 offset:112\n\t" \
    "ds_write_b128 v177, v[166:169]\n\t" \
    "v_lshlrev_b32 v162, 16, v144\n\t" \
    "v_and_b32 v163, 0xffff0000, v144\n\t" \
    "v_pk_mul_f32 v[162:163], v[162:163], v[160:161] op_sel_hi:[1,0]\n\t" \
    "ds_write_b16 v179, v154 offset:128\n\t" \
    "v_cvt_pk_bf16_f32 v170, v162, v163\n\t" \
    "ds_write_b16_d16_hi v179, v154 offset:144\n\t" \
    "v_lshlrev_b32 v164, 16, v145\n\t" \
    "v_and_b32 v165, 0xffff0000, v145\n\t" \
    "v_pk_mul_f32 v[164:165], v[164:165], v[160:161] op_sel_hi:[1,0]\n\t" \
    "ds_write_b16 v179, v155 offset:160\n\t" \
    "v_cvt_pk_bf16_f32 v171, v164, v165\n\t" \
    "ds_write_b16_d16_hi v179, v155 offset:176\n\t" \
    "v_lshlrev_b32 v186, 16, v146\n\t" \
    "v_and_b32 v187, 0xffff0000, v146\n\t" \
    "v_pk_mul_f32 v[186:187], v[186:187], v[160:161] op_sel_hi:[1,0]\n\t" \
    "ds_write_b16 v179, v156 offset:192\n\t" \
    "v_cvt_pk_bf16_f32 v172, v186, v187\n\t" \
    "ds_write_b16_d16_hi v179, v156 offset:208\n\t" \
    "v_lshlrev_b32 v188, 16, v147\n\t" \
    "v_and_b32 v189, 0xffff0000, v147\n\t" \
    "v_pk_mul_f32 v[188:189], v[188:189], v[160:161] op_sel_hi:[1,0]\n\t" \
    "ds_write_b16 v179, v157 offset:224\n\t" \
    "v_cvt_pk_bf16_f32 v173, v188, v189\n\t" \
    "ds_write_b16_d16_hi v179, v157 offset:240\n\t" \
    "ds_write_b128 v177, v[170:173] offset:512\n\t" \
    "v_lshlrev_b32 v162, 16, v148\n\t" \
    "v_and_b32 v163, 0xffff0000, v148\n\t" \
    "v_pk_mul_f32 v[162:163], v[162:163], v[160:161] op_sel_hi:[1,0]\n\t" \
    "ds_write_b16 v179, v149 offset:256\n\t" \
    "v_cvt_pk_bf16_f32 v174, v162, v163\n\t" \
    "ds_write_b16_d16_hi v179, v149 offset:272\n\t" \
    "ds_write_b16 v179, v123 offset:288\n\t" \
    "ds_write_b32 v178, v174\n\t" \
    "v_add_u32 v177, 0x4000, v177\n\t" \
    "v_add_u32 v178, 0x800, v178\n\t" \
    "v_add_u32 v179, 0x4c00, v179\n\t" \
    "s_waitcnt vmcnt(1)\n\t" \
    "v_dot2_f32_bf16 v82, v64, v64, 0\n\t" \
    "v_dot2_f32_bf16 v83, v65, v65, 0\n\t" \
    "v_dot2_f32_bf16 v82, v66, v66, v82\n\t" \
    "v_dot2_f32_bf16 v83, v67, v67, v83\n\t" \
    "v_dot2_f32_bf16 v82, v68, v68, v82\n\t" \
    "v_dot2_f32_bf16 v83, v69, v69, v83\n\t" \
    "v_dot2_f32_bf16 v82, v70, v70, v82\n\t" \
    "v_dot2_f32_bf16 v83, v71, v71, v83\n\t" \
    "v_dot2_f32_bf16 v82, v72, v72, v82\n\t" \
    "v_lshlrev_b32 v74, 16, v64\n\t" \
    "v_and_b32 v75, 0xffff0000, v64\n\t" \
    "v_lshlrev_b32 v76, 16, v65\n\t" \
    "v_and_b32 v77, 0xffff0000, v65\n\t" \
    "v_add_f32 v82, v82, v83\n\t" \
    "v_sqrt_f32 v82, v82\n\t" \
    "v_mov_b32 v85, 0xc2000000\n\t" \
    "v_add_f32 v82, 0x358637bd, v82\n\t" \
    "v_rcp_f32 v82, v82\n\t" \
    "v_mov_b32 v86, 0xbf820000\n\t" \
    "v_fma_f32 v85, abs(v159), v86, v85\n\t" \
    "v_mul_f32 v82, v159, v82\n\t" \
    "v_cvt_pk_bf16_f32 v121, v85, 0\n\t" \
    "v_pk_mul_f32 v[74:75], v[74:75], v[82:83] op_sel_hi:[1,0]\n\t" \
    "v_cvt_pk_bf16_f32 v112, v74, v75\n\t" \
    "v_pk_mul_f32 v[76:77], v[76:77], v[82:83] op_sel_hi:[1,0]\n\t" \
    "v_cvt_pk_bf16_f32 v113, v76, v77\n\t" \
    "v_lshlrev_b32 v78, 16, v66\n\t" \
    "v_and_b32 v79, 0xffff0000, v66\n\t" \
    "v_pk_mul_f32 v[78:79], v[78:79], v[82:83] op_sel_hi:[1,0]\n\t" \
    "v_cvt_pk_bf16_f32 v114, v78, v79\n\t" \
    "v_lshlrev_b32 v80, 16, v67\n\t" \
    "v_and_b32 v81, 0xffff0000, v67\n\t" \
    "v_pk_mul_f32 v[80:81], v[80:81], v[82:83] op_sel_hi:[1,0]\n\t" \
    "v_cvt_pk_bf16_f32 v115, v80, v81\n\t" \
    "v_lshlrev_b32 v74, 16, v68\n\t" \
    "v_and_b32 v75, 0xffff0000, v68\n\t" \
    "v_pk_mul_f32 v[74:75], v[74:75], v[82:83] op_sel_hi:[1,0]\n\t" \
    "v_cvt_pk_bf16_f32 v116, v74, v75\n\t" \
    "v_lshlrev_b32 v76, 16, v69\n\t" \
    "v_and_b32 v77, 0xffff0000, v69\n\t" \
    "v_pk_mul_f32 v[76:77], v[76:77], v[82:83] op_sel_hi:[1,0]\n\t" \
    "v_cvt_pk_bf16_f32 v117, v76, v77\n\t" \
    "v_lshlrev_b32 v78, 16, v70\n\t" \
    "v_and_b32 v79, 0xffff0000, v70\n\t" \
    "v_pk_mul_f32 v[78:79], v[78:79], v[82:83] op_sel_hi:[1,0]\n\t" \
    "v_cvt_pk_bf16_f32 v118, v78, v79\n\t" \
    "v_lshlrev_b32 v80, 16, v71\n\t" \
    "v_and_b32 v81, 0xffff0000, v71\n\t" \
    "v_pk_mul_f32 v[80:81], v[80:81], v[82:83] op_sel_hi:[1,0]\n\t" \
    "v_cvt_pk_bf16_f32 v119, v80, v81\n\t" \
    "v_lshlrev_b32 v74, 16, v72\n\t" \
    "v_and_b32 v75, 0xffff0000, v72\n\t" \
    "v_pk_mul_f32 v[74:75], v[74:75], v[82:83] op_sel_hi:[1,0]\n\t" \
    "v_cvt_pk_bf16_f32 v120, v74, v75\n\t" \
    "v_permlane32_swap_b32 v112, v116\n\t" \
    "v_permlane32_swap_b32 v113, v117\n\t" \
    "v_permlane32_swap_b32 v114, v118\n\t" \
    "v_permlane32_swap_b32 v115, v119\n\t" \
    "s_waitcnt lgkmcnt(0)\n\t" \
    "s_barrier\n\t" \
    "ds_read_b32 v122, v138 offset:256\n\t" \
    "ds_read_b128 v[124:127], v137 offset:2048\n\t" \
    "ds_read_b32 v158, v138 offset:384\n\t" \
    "ds_read_b128 v[166:169], v137 offset:3072\n\t" \
    "ds_read_b128 v[128:131], v139 offset:2432\n\t" \
    "v_add_u32 v176, 0x4800, v176\n\t" \
    "s_waitcnt lgkmcnt(4)\n\t" \
    "v_mfma_f32_32x32x4_2b_bf16 v[32:63], v[122:123], v[120:121], 0\n\t" \
    "s_mov_b32 %[j], 0\n\t" \
    "global_load_dwordx4 v[140:143], v176, %[kp] \n\t" \
    "global_load_dwordx4 v[144:147], v176, %[kp] offset:16 \n\t" \
    "global_load_dword v148, v176, %[kp] offset:32 \n\t" \
    "global_load_dwordx4 v[150:153], v176, %[vp] \n\t" \
    "global_load_dwordx4 v[154:157], v176, %[vp] offset:16 \n\t" \
    "global_load_dword v149, v176, %[vp] offset:32 \n\t" \
    "s_nop 9\n\t" \
    "s_waitcnt lgkmcnt(2)\n\t" \
    "v_mov_b32 v122, v158\n\t" \
    "v_mfma_f32_32x32x16_bf16 v[32:47], v[124:127], v[112:115], v[32:47]\n\t" \
    "v_mfma_f32_32x32x16_bf16 v[48:63], v[124:127], v[116:119], v[48:63]\n\t" \
    "s_waitcnt lgkmcnt(0)\n\t" \
    "v_mov_b32 v124, v166\n\t" \
    "v_mov_b32 v125, v167\n\t" \
    "v_mov_b32 v126, v168\n\t" \
    "v_mov_b32 v127, v169\n\t" \
    "s_mov_b64 vcc, 0\n\t" \
    "s_nop 2\n\t" \
    "s_branch Lent2_%=\n\t" \
    "Lplain_%=:\n\t" \
    "s_setprio 3\n\t" \
    "s_waitcnt lgkmcnt(3)\n\t" \
    "v_mfma_f32_32x32x4_2b_bf16 v[64:95], v[122:123], v[120:121], 0\n\t" \
    "ds_read_b32 v122, v138 offset:256\n\t" \
    "v_exp_f32 v32, v32\n\t" \
    "v_exp_f32 v33, v33\n\t" \
    "v_exp_f32 v34, v34\n\t" \
    "v_exp_f32 v35, v35\n\t" \
    "v_exp_f32 v36, v36\n\t" \
    "v_exp_f32 v37, v37\n\t" \
    "v_exp_f32 v38, v38\n\t" \
    "v_exp_f32 v39, v39\n\t" \
    "s_waitcnt lgkmcnt(3)\n\t" \
    "v_smfmac_f32_32x32x32_bf16 v[0:15], v[132:135], v[96:103], v136\n\t" \
    "v_exp_f32 v48, v48\n\t" \
    "v_exp_f32 v49, v49\n\t" \
    "v_exp_f32 v50, v50\n\t" \
    "v_exp_f32 v51, v51\n\t" \
    "v_smfmac_f32_32x32x32_bf16 v[16:31], v[132:135], v[104:111], v136\n\t" \
    "ds_read_b128 v[132:135], v139 offset:608\n\t" \
    "v_exp_f32 v52, v52\n\t" \
    "v_exp_f32 v53, v53\n\t" \
    "v_exp_f32 v54, v54\n\t" \
    "v_exp_f32 v55, v55\n\t" \
    "s_waitcnt lgkmcnt(3)\n\t" \
    "v_mfma_f32_32x32x16_bf16 v[64:79], v[124:127], v[112:115], v[64:79]\n\t" \
    "v_exp_f32 v96, v40\n\t" \
    "v_exp_f32 v97, v41\n\t" \
    "v_exp_f32 v98, v42\n\t" \
    "v_exp_f32 v99, v43\n\t" \
    "v_mfma_f32_32x32x16_bf16 v[80:95], v[124:127], v[116:119], v[80:95]\n\t" \
    "ds_read_b128 v[124:127], v137 offset:2048\n\t" \
    "v_exp_f32 v100, v44\n\t" \
    "v_exp_f32 v101, v45\n\t" \
    "v_exp_f32 v102, v46\n\t" \
    "v_exp_f32 v103, v47\n\t" \
    "s_waitcnt lgkmcnt(3)\n\t" \
    "v_smfmac_f32_32x32x32_bf16 v[0:15], v[128:131], v[32:39], v136\n\t" \
    "v_exp_f32 v104, v56\n\t" \
    "v_exp_f32 v105, v57\n\t" \
    "v_exp_f32 v106, v58\n\t" \
    "v_exp_f32 v107, v59\n\t" \
    "v_smfmac_f32_32x32x32_bf16 v[16:31], v[128:131], v[48:55], v136\n\t" \
    "ds_read_b128 v[128:131], v139 offset:1216\n\t" \
    "v_exp_f32 v108, v60\n\t" \
    "v_exp_f32 v109, v61\n\t" \
    "v_exp_f32 v110, v62\n\t" \
    "v_exp_f32 v111, v63\n\t" \
    "s_setprio 2\n\t" \
    "s_waitcnt lgkmcnt(3)\n\t" \
    "v_mfma_f32_32x32x4_2b_bf16 v[32:63], v[122:123], v[120:121], 0\n\t" \
    "ds_read_b32 v122, v138 offset:384\n\t" \
    "v_exp_f32 v64, v64\n\t" \
    "v_exp_f32 v65, v65\n\t" \
    "v_exp_f32 v66, v66\n\t" \
    "v_exp_f32 v67, v67\n\t" \
    "v_exp_f32 v68, v68\n\t" \
    "v_exp_f32 v69, v69\n\t" \
    "v_exp_f32 v70, v70\n\t" \
    "v_exp_f32 v71, v71\n\t" \
    "s_waitcnt lgkmcnt(3)\n\t" \
    "v_smfmac_f32_32x32x32_bf16 v[0:15], v[132:135], v[96:103], v136\n\t" \
    "v_exp_f32 v80, v80\n\t" \
    "v_exp_f32 v81, v81\n\t" \
    "v_exp_f32 v82, v82\n\t" \
    "v_exp_f32 v83, v83\n\t" \
    "v_smfmac_f32_32x32x32_bf16 v[16:31], v[132:135], v[104:111], v136\n\t" \
    "ds_read_b128 v[132:135], v139 offset:1824\n\t" \
    "v_exp_f32 v84, v84\n\t" \
    "v_exp_f32 v85, v85\n\t" \
    "v_exp_f32 v86, v86\n\t" \
    "v_exp_f32 v87, v87\n\t" \
    "s_waitcnt lgkmcnt(3)\n\t" \
    "v_mfma_f32_32x32x16_bf16 v[32:47], v[124:127], v[112:115], v[32:47]\n\t" \
    "v_exp_f32 v96, v72\n\t" \
    "v_exp_f32 v97, v73\n\t" \
    "v_exp_f32 v98, v74\n\t" \
    "v_exp_f32 v99, v75\n\t" \
    "v_mfma_f32_32x32x16_bf16 v[48:63], v[124:127], v[116:119], v[48:63]\n\t" \
    "ds_read_b128 v[124:127], v137 offset:3072\n\t" \
    "v_exp_f32 v100, v76\n\t" \
    "v_exp_f32 v101, v77\n\t" \
    "v_exp_f32 v102, v78\n\t" \
    "v_exp_f32 v103, v79\n\t" \
    "s_waitcnt lgkmcnt(3)\n\t" \
    "v_smfmac_f32_32x32x32_bf16 v[0:15], v[128:131], v[64:71], v136\n\t" \
    "v_exp_f32 v104, v88\n\t" \
    "v_exp_f32 v105, v89\n\t" \
    "v_exp_f32 v106, v90\n\t" \
    "v_exp_f32 v107, v91\n\t" \
    "v_smfmac_f32_32x32x32_bf16 v[16:31], v[128:131], v[80:87], v136\n\t" \
    "ds_read_b128 v[128:131], v139 offset:2432\n\t" \
    "v_exp_f32 v108, v92\n\t" \
    "v_exp_f32 v109, v93\n\t" \
    "v_exp_f32 v110, v94\n\t" \
    "v_exp_f32 v111, v95\n\t" \
    "s_cmp_eq_u32 %[j], 16\n\t" \
    "s_cbranch_scc1 Ldone_%=\n\t" \
    "Lent2_%=:\n\t" \
    "s_setprio 1\n\t" \
    "s_waitcnt lgkmcnt(3)\n\t" \
    "v_mfma_f32_32x32x4_2b_bf16 v[64:95], v[122:123], v[120:121], 0\n\t" \
    "ds_read_b32 v122, v138 offset:512\n\t" \
    "v_exp_f32 v32, v32\n\t" \
    "v_exp_f32 v33, v33\n\t" \
    "v_exp_f32 v34, v34\n\t" \
    "v_exp_f32 v35, v35\n\t" \
    "v_exp_f32 v36, v36\n\t" \
    "v_exp_f32 v37, v37\n\t" \
    "v_exp_f32 v38, v38\n\t" \
    "v_exp_f32 v39, v39\n\t" \
    "s_waitcnt lgkmcnt(3)\n\t" \
    "v_smfmac_f32_32x32x32_bf16 v[0:15], v[132:135], v[96:103], v136\n\t" \
    "v_exp_f32 v48, v48\n\t" \
    "v_exp_f32 v49, v49\n\t" \
    "v_exp_f32 v50, v50\n\t" \
    "v_exp_f32 v51, v51\n\t" \
    "v_smfmac_f32_32x32x32_bf16 v[16:31], v[132:135], v[104:111], v136\n\t" \
    "ds_read_b128 v[132:135], v139 offset:3040\n\t" \
    "v_exp_f32 v52, v52\n\t" \
    "v_exp_f32 v53, v53\n\t" \
    "v_exp_f32 v54, v54\n\t" \
    "v_exp_f32 v55, v55\n\t" \
    "s_waitcnt lgkmcnt(3)\n\t" \
    "v_mfma_f32_32x32x16_bf16 v[64:79], v[124:127], v[112:115], v[64:79]\n\t" \
    "v_exp_f32 v96, v40\n\t" \
    "v_exp_f32 v97, v41\n\t" \
    "v_exp_f32 v98, v42\n\t" \
    "v_exp_f32 v99, v43\n\t" \
    "v_mfma_f32_32x32x16_bf16 v[80:95], v[124:127], v[116:119], v[80:95]\n\t" \
    "ds_read_b128 v[124:127], v137 offset:4096\n\t" \
    "v_exp_f32 v100, v44\n\t" \
    "v_exp_f32 v101, v45\n\t" \
    "v_exp_f32 v102, v46\n\t" \
    "v_exp_f32 v103, v47\n\t" \
    "s_waitcnt lgkmcnt(3)\n\t" \
    "v_smfmac_f32_32x32x32_bf16 v[0:15], v[128:131], v[32:39], v136\n\t" \
    "v_exp_f32 v104, v56\n\t" \
    "v_exp_f32 v105, v57\n\t" \
    "v_exp_f32 v106, v58\n\t" \
    "v_exp_f32 v107, v59\n\t" \
    "v_smfmac_f32_32x32x32_bf16 v[16:31], v[128:131], v[48:55], v136\n\t" \
    "ds_read_b128 v[128:131], v139 offset:3648\n\t" \
    "v_exp_f32 v108, v60\n\t" \
    "v_exp_f32 v109, v61\n\t" \
    "v_exp_f32 v110, v62\n\t" \
    "v_exp_f32 v111, v63\n\t" \
    "s_setprio 0\n\t" \
    "s_waitcnt lgkmcnt(3)\n\t" \
    "v_mfma_f32_32x32x4_2b_bf16 v[32:63], v[122:123], v[120:121], 0\n\t" \
    "ds_read_b32 v122, v138 offset:640\n\t" \
    "v_exp_f32 v64, v64\n\t" \
    "v_exp_f32 v65, v65\n\t" \
    "v_exp_f32 v66, v66\n\t" \
    "v_exp_f32 v67, v67\n\t" \
    "v_exp_f32 v68, v68\n\t" \
    "v_exp_f32 v69, v69\n\t" \
    "v_exp_f32 v70, v70\n\t" \
    "v_exp_f32 v71, v71\n\t" \
    "s_waitcnt lgkmcnt(3)\n\t" \
    "v_smfmac_f32_32x32x32_bf16 v[0:15], v[132:135], v[96:103], v136\n\t" \
    "v_exp_f32 v80, v80\n\t" \
    "v_exp_f32 v81, v81\n\t" \
    "v_exp_f32 v82, v82\n\t" \
    "v_exp_f32 v83, v83\n\t" \
    "v_smfmac_f32_32x32x32_bf16 v[16:31], v[132:135], v[104:111], v136\n\t" \
    "ds_read_b128 v[132:135], v139 offset:4256\n\t" \
    "v_exp_f32 v84, v84\n\t" \
    "v_exp_f32 v85, v85\n\t" \
    "v_exp_f32 v86, v86\n\t" \
    "v_exp_f32 v87, v87\n\t" \
    "s_waitcnt lgkmcnt(3)\n\t" \
    "v_mfma_f32_32x32x16_bf16 v[32:47], v[124:127], v[112:115], v[32:47]\n\t" \
    "v_exp_f32 v96, v72\n\t" \
    "v_exp_f32 v97, v73\n\t" \
    "v_exp_f32 v98, v74\n\t" \
    "v_exp_f32 v99, v75\n\t" \
    "v_mfma_f32_32x32x16_bf16 v[48:63], v[124:127], v[116:119], v[48:63]\n\t" \
    "ds_read_b128 v[124:127], v137 offset:5120\n\t" \
    "v_exp_f32 v100, v76\n\t" \
    "v_exp_f32 v101, v77\n\t" \
    "v_exp_f32 v102, v78\n\t" \
    "v_exp_f32 v103, v79\n\t" \
    "s_waitcnt lgkmcnt(3)\n\t" \
    "v_smfmac_f32_32x32x32_bf16 v[0:15], v[128:131], v[64:71], v136\n\t" \
    "v_exp_f32 v104, v88\n\t" \
    "v_exp_f32 v105, v89\n\t" \
    "v_exp_f32 v106, v90\n\t" \
    "v_exp_f32 v107, v91\n\t" \
    "v_smfmac_f32_32x32x32_bf16 v[16:31], v[128:131], v[80:87], v136\n\t" \
    "ds_read_b128 v[128:131], v139 offset:4864\n\t" \
    "v_exp_f32 v108, v92\n\t" \
    "v_exp_f32 v109, v93\n\t" \
    "v_exp_f32 v110, v94\n\t" \
    "v_exp_f32 v111, v95\n\t" \
    "v_add_u32 v138, 512, v138\n\t" \
    "v_add_u32 v137, 4096, v137\n\t" \
    "v_add_u32 v139, 4864, v139\n\t" \
    "s_add_u32 %[j], %[j], 1\n\t" \
    "s_and_b32 %[t], %[j], 3\n\t" \
    "s_cmp_lg_u32 %[t], 3\n\t" \
    "s_cbranch_scc1 Lplain_%=\n\t" \
    "s_cmp_eq_u32 %[j], 15\n\t" \
    "s_cbranch_scc1 Lplain_%=\n\t" \
    "s_setprio 3\n\t" \
    "s_waitcnt lgkmcnt(3)\n\t" \
    "v_mfma_f32_32x32x4_2b_bf16 v[64:95], v[122:123], v[120:121], 0\n\t" \
    "ds_read_b32 v122, v138 offset:256\n\t" \
    "v_exp_f32 v32, v32\n\t" \
    "v_exp_f32 v33, v33\n\t" \
    "v_exp_f32 v34, v34\n\t" \
    "v_exp_f32 v35, v35\n\t" \
    "v_exp_f32 v36, v36\n\t" \
    "v_exp_f32 v37, v37\n\t" \
    "v_exp_f32 v38, v38\n\t" \
    "v_exp_f32 v39, v39\n\t" \
    "s_waitcnt lgkmcnt(3)\n\t" \
    "v_smfmac_f32_32x32x32_bf16 v[0:15], v[132:135], v[96:103], v136\n\t" \
    "v_exp_f32 v48, v48\n\t" \
    "v_exp_f32 v49, v49\n\t" \
    "v_exp_f32 v50, v50\n\t" \
    "v_exp_f32 v51, v51\n\t" \
    "v_smfmac_f32_32x32x32_bf16 v[16:31], v[132:135], v[104:111], v136\n\t" \
    "ds_read_b128 v[132:135], v139 offset:608\n\t" \
    "v_exp_f32 v52, v52\n\t" \
    "v_exp_f32 v53, v53\n\t" \
    "v_exp_f32 v54, v54\n\t" \
    "v_exp_f32 v55, v55\n\t" \
    "s_waitcnt lgkmcnt(3)\n\t" \
    "v_mfma_f32_32x32x16_bf16 v[64:79], v[124:127], v[112:115], v[64:79]\n\t" \
    "v_exp_f32 v96, v40\n\t" \
    "v_exp_f32 v97, v41\n\t" \
    "v_exp_f32 v98, v42\n\t" \
    "v_exp_f32 v99, v43\n\t" \
    "v_mfma_f32_32x32x16_bf16 v[80:95], v[124:127], v[116:119], v[80:95]\n\t" \
    "ds_read_b128 v[124:127], v137 offset:2048\n\t" \
    "v_exp_f32 v100, v44\n\t" \
    "v_exp_f32 v101, v45\n\t" \
    "v_exp_f32 v102, v46\n\t" \
    "v_exp_f32 v103, v47\n\t" \
    "s_waitcnt lgkmcnt(3)\n\t" \
    "v_smfmac_f32_32x32x32_bf16 v[0:15], v[128:131], v[32:39], v136\n\t" \
    "v_exp_f32 v104, v56\n\t" \
    "v_exp_f32 v105, v57\n\t" \
    "v_exp_f32 v106, v58\n\t" \
    "v_exp_f32 v107, v59\n\t" \
    "s_waitcnt vmcnt(0)\n\t" \
    "v_dot2_f32_bf16 v158, v140, v140, 0\n\t" \
    "v_dot2_f32_bf16 v158, v141, v141, v158\n\t" \
    "v_dot2_f32_bf16 v158, v142, v142, v158\n\t" \
    "v_dot2_f32_bf16 v158, v143, v143, v158\n\t" \
    "v_dot2_f32_bf16 v158, v144, v144, v158\n\t" \
    "v_dot2_f32_bf16 v158, v145, v145, v158\n\t" \
    "v_dot2_f32_bf16 v158, v146, v146, v158\n\t" \
    "v_dot2_f32_bf16 v158, v147, v147, v158\n\t" \
    "v_dot2_f32_bf16 v158, v148, v148, v158\n\t" \
    "ds_write_b16 v179, v150 offset:0\n\t" \
    "ds_write_b16_d16_hi v179, v150 offset:16\n\t" \
    "ds_write_b16 v179, v151 offset:32\n\t" \
    "v_sqrt_f32 v158, v158\n\t" \
    "ds_write_b16_d16_hi v179, v151 offset:48\n\t" \
    "v_add_f32 v158, 0x358637bd, v158\n\t" \
    "v_rcp_f32 v160, v158\n\t" \
    "ds_write_b16 v179, v152 offset:64\n\t" \
    "v_lshlrev_b32 v198, 16, v140\n\t" \
    "v_and_b32 v199, 0xffff0000, v140\n\t" \
    "v_mul_f32 v198, v160, v198\n\t" \
    "v_mul_f32 v199, v160, v199\n\t" \
    "ds_write_b16_d16_hi v179, v152 offset:80\n\t" \
    "v_cvt_pk_bf16_f32 v166, v198, v199\n\t" \
    "ds_write_b16 v179, v153 offset:96\n\t" \
    "v_lshlrev_b32 v200, 16, v141\n\t" \
    "v_and_b32 v201, 0xffff0000, v141\n\t" \
    "v_mul_f32 v200, v160, v200\n\t" \
    "v_mul_f32 v201, v160, v201\n\t" \
    "ds_write_b16_d16_hi v179, v153 offset:112\n\t" \
    "v_cvt_pk_bf16_f32 v167, v200, v201\n\t" \
    "ds_write_b16 v179, v154 offset:128\n\t" \
    "v_lshlrev_b32 v202, 16, v142\n\t" \
    "v_and_b32 v203, 0xffff0000, v142\n\t" \
    "v_mul_f32 v202, v160, v202\n\t" \
    "v_mul_f32 v203, v160, v203\n\t" \
    "ds_write_b16_d16_hi v179, v154 offset:144\n\t" \
    "v_cvt_pk_bf16_f32 v168, v202, v203\n\t" \
    "ds_write_b16 v179, v155 offset:160\n\t" \
    "v_lshlrev_b32 v204, 16, v143\n\t" \
    "v_and_b32 v205, 0xffff0000, v143\n\t" \
    "v_mul_f32 v204, v160, v204\n\t" \
    "v_mul_f32 v205, v160, v205\n\t" \
    "ds_write_b16_d16_hi v179, v155 offset:176\n\t" \
    "v_cvt_pk_bf16_f32 v169, v204, v205\n\t" \
    "ds_write_b16 v179, v156 offset:192\n\t" \
    "ds_write_b128 v177, v[166:169]\n\t" \
    "v_lshlrev_b32 v206, 16, v144\n\t" \
    "v_and_b32 v207, 0xffff0000, v144\n\t" \
    "v_mul_f32 v206, v160, v206\n\t" \
    "v_mul_f32 v207, v160, v207\n\t" \
    "ds_write_b16_d16_hi v179, v156 offset:208\n\t" \
    "v_cvt_pk_bf16_f32 v170, v206, v207\n\t" \
    "ds_write_b16 v179, v157 offset:224\n\t" \
    "v_lshlrev_b32 v208, 16, v145\n\t" \
    "v_and_b32 v209, 0xffff0000, v145\n\t" \
    "v_mul_f32 v208, v160, v208\n\t" \
    "v_mul_f32 v209, v160, v209\n\t" \
    "ds_write_b16_d16_hi v179, v157 offset:240\n\t" \
    "v_cvt_pk_bf16_f32 v171, v208, v209\n\t" \
    "ds_write_b16 v179, v149 offset:256\n\t" \
    "v_lshlrev_b32 v210, 16, v146\n\t" \
    "v_and_b32 v211, 0xffff0000, v146\n\t" \
    "v_mul_f32 v210, v160, v210\n\t" \
    "v_mul_f32 v211, v160, v211\n\t" \
    "ds_write_b16_d16_hi v179, v149 offset:272\n\t" \
    "v_cvt_pk_bf16_f32 v172, v210, v211\n\t" \
    "ds_write_b16 v179, v123 offset:288\n\t" \
    "v_lshlrev_b32 v212, 16, v147\n\t" \
    "v_and_b32 v213, 0xffff0000, v147\n\t" \
    "v_mul_f32 v212, v160, v212\n\t" \
    "v_mul_f32 v213, v160, v213\n\t" \
    "v_cvt_pk_bf16_f32 v173, v212, v213\n\t" \
    "ds_write_b128 v177, v[170:173] offset:512\n\t" \
    "v_lshlrev_b32 v214, 16, v148\n\t" \
    "v_and_b32 v215, 0xffff0000, v148\n\t" \
    "v_mul_f32 v214, v160, v214\n\t" \
    "v_mul_f32 v215, v160, v215\n\t" \
    "v_cvt_pk_bf16_f32 v174, v214, v215\n\t" \
    "ds_write_b32 v178, v174\n\t" \
    "v_smfmac_f32_32x32x32_bf16 v[16:31], v[128:131], v[48:55], v136\n\t" \
    "ds_read_b128 v[128:131], v139 offset:1216\n\t" \
    "v_exp_f32 v108, v60\n\t" \
    "v_exp_f32 v109, v61\n\t" \
    "v_exp_f32 v110, v62\n\t" \
    "v_exp_f32 v111, v63\n\t" \
    "s_cmp_ge_u32 %[j], 11\n\t" \
    "s_cbranch_scc1 Lnofetch_%=\n\t" \
    "v_add_u32 v176, 0x4800, v176\n\t" \
    "global_load_dwordx4 v[140:143], v176, %[kp] \n\t" \
    "global_load_dwordx4 v[144:147], v176, %[kp] offset:16 \n\t" \
    "global_load_dword v148, v176, %[kp] offset:32 \n\t" \
    "global_load_dwordx4 v[150:153], v176, %[vp] \n\t" \
    "global_load_dwordx4 v[154:157], v176, %[vp] offset:16 \n\t" \
    "global_load_dword v149, v176, %[vp] offset:32 \n\t" \
    "Lnofetch_%=:\n\t" \
    "s_setprio 2\n\t" \
    "s_waitcnt lgkmcnt(15)\n\t" \
    "v_mfma_f32_32x32x4_2b_bf16 v[32:63], v[122:123], v[120:121], 0\n\t" \
    "ds_read_b32 v122, v138 offset:384\n\t" \
    "v_exp_f32 v64, v64\n\t" \
    "v_exp_f32 v65, v65\n\t" \
    "v_add_u32 v177, 0x4000, v177\n\t" \
    "v_exp_f32 v66, v66\n\t" \
    "v_add_u32 v178, 0x800, v178\n\t" \
    "v_exp_f32 v67, v67\n\t" \
    "v_add_u32 v179, 0x4c00, v179\n\t" \
    "v_exp_f32 v68, v68\n\t" \
    "v_exp_f32 v69, v69\n\t" \
    "v_exp_f32 v70, v70\n\t" \
    "v_exp_f32 v71, v71\n\t" \
    "s_waitcnt lgkmcnt(15)\n\t" \
    "v_smfmac_f32_32x32x32_bf16 v[0:15], v[132:135], v[96:103], v136\n\t" \
    "v_exp_f32 v80, v80\n\t" \
    "v_exp_f32 v81, v81\n\t" \
    "v_exp_f32 v82, v82\n\t" \
    "v_exp_f32 v83, v83\n\t" \
    "v_smfmac_f32_32x32x32_bf16 v[16:31], v[132:135], v[104:111], v136\n\t" \
    "ds_read_b128 v[132:135], v139 offset:1824\n\t" \
    "v_exp_f32 v84, v84\n\t" \
    "v_exp_f32 v85, v85\n\t" \
    "v_exp_f32 v86, v86\n\t" \
    "v_exp_f32 v87, v87\n\t" \
    "s_waitcnt lgkmcnt(15)\n\t" \
    "v_mfma_f32_32x32x16_bf16 v[32:47], v[124:127], v[112:115], v[32:47]\n\t" \
    "v_exp_f32 v96, v72\n\t" \
    "v_exp_f32 v97, v73\n\t" \
    "v_exp_f32 v98, v74\n\t" \
    "v_exp_f32 v99, v75\n\t" \
    "v_mfma_f32_32x32x16_bf16 v[48:63], v[124:127], v[116:119], v[48:63]\n\t" \
    "ds_read_b128 v[124:127], v137 offset:3072\n\t" \
    "v_exp_f32 v100, v76\n\t" \
    "v_exp_f32 v101, v77\n\t" \
    "v_exp_f32 v102, v78\n\t" \
    "v_exp_f32 v103, v79\n\t" \
    "s_waitcnt lgkmcnt(3)\n\t" \
    "v_smfmac_f32_32x32x32_bf16 v[0:15], v[128:131], v[64:71], v136\n\t" \
    "v_exp_f32 v104, v88\n\t" \
    "v_exp_f32 v105, v89\n\t" \
    "v_exp_f32 v106, v90\n\t" \
    "v_exp_f32 v107, v91\n\t" \
    "v_smfmac_f32_32x32x32_bf16 v[16:31], v[128:131], v[80:87], v136\n\t" \
    "ds_read_b128 v[128:131], v139 offset:2432\n\t" \
    "v_exp_f32 v108, v92\n\t" \
    "v_exp_f32 v109, v93\n\t" \
    "v_exp_f32 v110, v94\n\t" \
    "v_exp_f32 v111, v95\n\t" \
    "s_setprio 1\n\t" \
    "s_waitcnt lgkmcnt(3)\n\t" \
    "v_mfma_f32_32x32x4_2b_bf16 v[64:95], v[122:123], v[120:121], 0\n\t" \
    "ds_read_b32 v122, v138 offset:512\n\t" \
    "v_exp_f32 v32, v32\n\t" \
    "v_exp_f32 v33, v33\n\t" \
    "v_exp_f32 v34, v34\n\t" \
    "v_exp_f32 v35, v35\n\t" \
    "v_exp_f32 v36, v36\n\t" \
    "v_exp_f32 v37, v37\n\t" \
    "v_exp_f32 v38, v38\n\t" \
    "v_exp_f32 v39, v39\n\t" \
    "s_waitcnt lgkmcnt(3)\n\t" \
    "v_smfmac_f32_32x32x32_bf16 v[0:15], v[132:135], v[96:103], v136\n\t" \
    "v_exp_f32 v48, v48\n\t" \
    "v_exp_f32 v49, v49\n\t" \
    "v_exp_f32 v50, v50\n\t" \
    "v_exp_f32 v51, v51\n\t" \
    "v_smfmac_f32_32x32x32_bf16 v[16:31], v[132:135], v[104:111], v136\n\t" \
    "ds_read_b128 v[132:135], v139 offset:3040\n\t" \
    "v_exp_f32 v52, v52\n\t" \
    "v_exp_f32 v53, v53\n\t" \
    "v_exp_f32 v54, v54\n\t" \
    "v_exp_f32 v55, v55\n\t" \
    "s_waitcnt lgkmcnt(3)\n\t" \
    "v_mfma_f32_32x32x16_bf16 v[64:79], v[124:127], v[112:115], v[64:79]\n\t" \
    "v_exp_f32 v96, v40\n\t" \
    "v_exp_f32 v97, v41\n\t" \
    "ds_write_b8 v180, v184\n\t" \
    "v_exp_f32 v98, v42\n\t" \
    "v_exp_f32 v99, v43\n\t" \
    "v_add_u32 v180, 8, v180\n\t" \
    "v_mfma_f32_32x32x16_bf16 v[80:95], v[124:127], v[116:119], v[80:95]\n\t" \
    "ds_read_b128 v[124:127], v137 offset:4096\n\t" \
    "v_exp_f32 v100, v44\n\t" \
    "v_exp_f32 v101, v45\n\t" \
    "v_exp_f32 v102, v46\n\t" \
    "v_exp_f32 v103, v47\n\t" \
    "s_waitcnt lgkmcnt(4)\n\t" \
    "v_smfmac_f32_32x32x32_bf16 v[0:15], v[128:131], v[32:39], v136\n\t" \
    "v_exp_f32 v104, v56\n\t" \
    "v_exp_f32 v105, v57\n\t" \
    "v_exp_f32 v106, v58\n\t" \
    "v_exp_f32 v107, v59\n\t" \
    "v_smfmac_f32_32x32x32_bf16 v[16:31], v[128:131], v[48:55], v136\n\t" \
    "ds_read_b128 v[128:131], v139 offset:3648\n\t" \
    "v_exp_f32 v108, v60\n\t" \
    "v_exp_f32 v109, v61\n\t" \
    "v_exp_f32 v110, v62\n\t" \
    "v_exp_f32 v111, v63\n\t" \
    "s_setprio 0\n\t" \
    "s_waitcnt lgkmcnt(4)\n\t" \
    "v_mfma_f32_32x32x4_2b_bf16 v[32:63], v[122:123], v[120:121], 0\n\t" \
    "ds_read_b32 v122, v138 offset:640\n\t" \
    "v_exp_f32 v64, v64\n\t" \
    "v_exp_f32 v65, v65\n\t" \
    "v_exp_f32 v66, v66\n\t" \
    "v_exp_f32 v67, v67\n\t" \
    "v_exp_f32 v68, v68\n\t" \
    "v_exp_f32 v69, v69\n\t" \
    "v_exp_f32 v70, v70\n\t" \
    "v_exp_f32 v71, v71\n\t" \
    "s_waitcnt lgkmcnt(4)\n\t" \
    "v_smfmac_f32_32x32x32_bf16 v[0:15], v[132:135], v[96:103], v136\n\t" \
    "v_exp_f32 v80, v80\n\t" \
    "v_exp_f32 v81, v81\n\t" \
    "v_exp_f32 v82, v82\n\t" \
    "v_exp_f32 v83, v83\n\t" \
    "v_smfmac_f32_32x32x32_bf16 v[16:31], v[132:135], v[104:111], v136\n\t" \
    "ds_read_b128 v[132:135], v139 offset:4256\n\t" \
    "v_exp_f32 v84, v84\n\t" \
    "v_exp_f32 v85, v85\n\t" \
    "v_exp_f32 v86, v86\n\t" \
    "v_exp_f32 v87, v87\n\t" \
    "s_waitcnt lgkmcnt(3)\n\t" \
    "v_mfma_f32_32x32x16_bf16 v[32:47], v[124:127], v[112:115], v[32:47]\n\t" \
    "v_exp_f32 v96, v72\n\t" \
    "v_exp_f32 v97, v73\n\t" \
    "v_exp_f32 v98, v74\n\t" \
    "v_exp_f32 v99, v75\n\t" \
    "v_mfma_f32_32x32x16_bf16 v[48:63], v[124:127], v[116:119], v[48:63]\n\t" \
    "ds_read_b128 v[124:127], v137 offset:5120\n\t" \
    "v_exp_f32 v100, v76\n\t" \
    "v_exp_f32 v101, v77\n\t" \
    "v_exp_f32 v102, v78\n\t" \
    "v_exp_f32 v103, v79\n\t" \
    "ds_read_b64 v[182:183], v181\n\t" \
    "s_waitcnt lgkmcnt(4)\n\t" \
    "v_smfmac_f32_32x32x32_bf16 v[0:15], v[128:131], v[64:71], v136\n\t" \
    "v_exp_f32 v104, v88\n\t" \
    "v_exp_f32 v105, v89\n\t" \
    "v_exp_f32 v106, v90\n\t" \
    "v_exp_f32 v107, v91\n\t" \
    "v_smfmac_f32_32x32x32_bf16 v[16:31], v[128:131], v[80:87], v136\n\t" \
    "ds_read_b128 v[128:131], v139 offset:4864\n\t" \
    "v_exp_f32 v108, v92\n\t" \
    "v_exp_f32 v109, v93\n\t" \
    "v_exp_f32 v110, v94\n\t" \
    "v_exp_f32 v111, v95\n\t" \
    "v_add_u32 v138, 512, v138\n\t" \
    "v_add_u32 v137, 4096, v137\n\t" \
    "v_add_u32 v139, 4864, v139\n\t" \
    "s_add_u32 %[j], %[j], 1\n\t" \
    "s_waitcnt lgkmcnt(1)\n\t" \
    "Lpoll_%=:\n\t" \
    "v_and_b32 v182, v182, v183\n\t" \
    "v_cmp_ne_u32 vcc, 0x01010101, v182\n\t" \
    "s_cbranch_vccz Lok_%=\n\t" \
    "s_sleep 1\n\t" \
    "ds_read_b64 v[182:183], v181\n\t" \
    "s_waitcnt lgkmcnt(0)\n\t" \
    "s_branch Lpoll_%=\n\t" \
    "Lok_%=:\n\t" \
    "v_add_u32 v181, 8, v181\n\t" \
    "s_branch Lplain_%=\n\t" \
    "Ldone_%=:\n\t" \
    "s_waitcnt lgkmcnt(0)\n\t" \
    "v_smfmac_f32_32x32x32_bf16 v[0:15], v[132:135], v[96:103], v136\n\t" \
    "v_smfmac_f32_32x32x32_bf16 v[16:31], v[132:135], v[104:111], v136\n\t" \
    "s_nop 11\n\t" \
    "v_permlane32_swap_b32 v10, v26\n\t" \
    "v_permlane32_swap_b32 v0, v16\n\t" \
    "v_permlane32_swap_b32 v1, v17\n\t" \
    "v_permlane32_swap_b32 v2, v18\n\t" \
    "v_permlane32_swap_b32 v3, v19\n\t" \
    "v_permlane32_swap_b32 v4, v20\n\t" \
    "v_permlane32_swap_b32 v5, v21\n\t" \
    "v_permlane32_swap_b32 v6, v22\n\t" \
    "v_permlane32_swap_b32 v7, v23\n\t" \
    "v_permlane32_swap_b32 v8, v24\n\t" \
    "v_permlane32_swap_b32 v9, v25\n\t" \
    "v_rcp_f32 v32, v10\n\t" \
    "s_nop 0\n\t" \
    "v_add_u32 v191, 8448, v191\n\t" \
    "v_pk_mul_f32 v[0:1], v[0:1], v[32:33] op_sel_hi:[1,0]\n\t" \
    "v_pk_mul_f32 v[2:3], v[2:3], v[32:33] op_sel_hi:[1,0]\n\t" \
    "v_cvt_pk_bf16_f32 v36, v0, v1\n\t" \
    "v_pk_mul_f32 v[16:17], v[16:17], v[32:33] op_sel_hi:[1,0]\n\t" \
    "v_cvt_pk_bf16_f32 v37, v2, v3\n\t" \
    "ds_write2_b32 v191, v36, v37 offset0:0 offset1:1\n\t" \
    "v_pk_mul_f32 v[18:19], v[18:19], v[32:33] op_sel_hi:[1,0]\n\t" \
    "v_cvt_pk_bf16_f32 v38, v16, v17\n\t" \
    "v_pk_mul_f32 v[4:5], v[4:5], v[32:33] op_sel_hi:[1,0]\n\t" \
    "v_cvt_pk_bf16_f32 v39, v18, v19\n\t" \
    "ds_write2_b32 v191, v38, v39 offset0:2 offset1:3\n\t" \
    "v_pk_mul_f32 v[6:7], v[6:7], v[32:33] op_sel_hi:[1,0]\n\t" \
    "v_cvt_pk_bf16_f32 v40, v4, v5\n\t" \
    "v_pk_mul_f32 v[20:21], v[20:21], v[32:33] op_sel_hi:[1,0]\n\t" \
    "v_cvt_pk_bf16_f32 v41, v6, v7\n\t" \
    "ds_write2_b32 v191, v40, v41 offset0:4 offset1:5\n\t" \
    "v_pk_mul_f32 v[22:23], v[22:23], v[32:33] op_sel_hi:[1,0]\n\t" \
    "v_cvt_pk_bf16_f32 v42, v20, v21\n\t" \
    "v_pk_mul_f32 v[8:9], v[8:9], v[32:33] op_sel_hi:[1,0]\n\t" \
    "v_cvt_pk_bf16_f32 v43, v22, v23\n\t" \
    "ds_write2_b32 v191, v42, v43 offset0:6 offset1:7\n\t" \
    "v_cvt_pk_bf16_f32 v44, v8, v9\n\t" \
    "ds_write_b32 v191, v44 offset:32\n\t" \
    "ds_read_b128 v[48:51], v185 offset:8448\n\t" \
    "ds_read_b128 v[52:55], v185 offset:9472\n\t" \
    "ds_read_b128 v[56:59], v185 offset:10496\n\t" \
    "s_waitcnt lgkmcnt(2)\n\t" \
    "global_store_dwordx4 v185, v[48:51], %[op] sc1\n\t" \
    "s_waitcnt lgkmcnt(1)\n\t" \
    "global_store_dwordx4 v185, v[52:55], %[op] offset:1024 sc1\n\t" \
    "s_mov_b64 exec, 0xffff\n\t" \
    "s_waitcnt lgkmcnt(0)\n\t" \
    "global_store_dwordx4 v185, v[56:59], %[op] offset:2048 sc1\n\t" \
    "s_mov_b64 exec, -1\n\t" \
    "s_mov_b32 %[done], 1\n\t" \
    "Lbail_%=:\n\t" \
    "s_cmp_lg_u32 %[done], 0\n\t" \
    "s_cbranch_scc1 Lend_%=\n\t" \
    "s_waitcnt vmcnt(0) lgkmcnt(0)\n\t" \
    "Lend_%=:\n\t" \
    "s_nop 1\n\t"
#define MAIN_TID(x) "{v190}"(x)
#define MAIN_CLOB "v0", "v1", "v2", "v3", "v4", "v5", "v6", "v7", "v8", "v9", "v10", "v11", "v12", "v13", "v14", "v15", "v16", "v17", "v18", "v19", "v20", "v21", "v22", "v23", "v24", "v25", "v26", "v27", "v28", "v29", "v30", "v31", "v32", "v33", "v34", "v35", "v36", "v37", "v38", "v39", "v40", "v41", "v42", "v43", "v44", "v45", "v46", "v47", "v48", "v49", "v50", "v51", "v52", "v53", "v54", "v55", "v56", "v57", "v58", "v59", "v60", "v61", "v62", "v63", "v64", "v65", "v66", "v67", "v68", "v69", "v70", "v71", "v72", "v73", "v74", "v75", "v76", "v77", "v78", "v79", "v80", "v81", "v82", "v83", "v84", "v85", "v86", "v87", "v88", "v89", "v90", "v91", "v92", "v93", "v94", "v95", "v96", "v97", "v98", "v99", "v100", "v101", "v102", "v103", "v104", "v105", "v106", "v107", "v108", "v109", "v110", "v111", "v112", "v113", "v114", "v115", "v116", "v117", "v118", "v119", "v120", "v121", "v122", "v123", "v124", "v125", "v126", "v127", "v128", "v129", "v130", "v131", "v132", "v133", "v134", "v135", "v136", "v137", "v138", "v139", "v140", "v141", "v142", "v143", "v144", "v145", "v146", "v147", "v148", "v149", "v150", "v151", "v152", "v153", "v154", "v155", "v156", "v157", "v158", "v159", "v160", "v161", "v162", "v163", "v164", "v165", "v166", "v167", "v168", "v169", "v170", "v171", "v172", "v173", "v174", "v175", "v176", "v177", "v178", "v179", "v180", "v181", "v182", "v183", "v184", "v185", "v186", "v187", "v188", "v189", "v191", "v192", "v193", "v194", "v195", "v196", "v197", "v198", "v199", "v200", "v201", "v202", "v203", "v204", "v205", "v206", "v207", "v208", "v209", "v210", "v211", "v212", "v213", "v214", "v215", "v216", "v217", "v218", "v219"


__global__ __launch_bounds__(512) void qknorm_attn(const uint16_t* __restrict__ Q, const uint16_t* __restrict__ K,
                                                   const uint16_t* __restrict__ V, const uint16_t* __restrict__ T,
                                                   uint16_t* __restrict__ O
                                                   ) {
    extern __shared__ __attribute__((aligned(16))) char smem[];
    const int tid = threadIdx.x;
    const int bid = blockIdx.x;
    const int bh = (bid & 7) + 8 * (bid >> 5), qc = (bid >> 3) & 3;
    const size_t hb = (size_t)bh * SEQ * DH;
    const char* ksrc = (const char*)(K + hb);
    const char* vsrc = (const char*)(V + hb);
    const char* qsrc = (const char*)(Q + hb) + qc * 512 * 36;
    char* odst = (char*)(O + hb) + qc * 512 * 36;
    const unsigned* tsrc = (const unsigned*)T + ((bh & (NHEAD - 1)) >> 1);
    {
        int done, j, t; unsigned long long pc;
        asm volatile(MAIN_ASM
            : [done] "=&s"(done), [j] "=&s"(j), [t] "=&s"(t), [pc] "=&s"(pc)
            : MAIN_TID(tid), [kp] "s"(ksrc), [vp] "s"(vsrc), [qp] "s"(qsrc), [op] "s"(odst), [tp] "s"(tsrc), [odd] "s"(bh)
            : "memory", "scc", "vcc", MAIN_CLOB);
        if (done) return;
    }
    const lds_ptr lds = (lds_ptr)smem;
    const int lane = tid & 63, r32 = lane & 31, hi = lane >> 5;
    const unsigned tw = *tsrc;
    const float cs = __uint_as_float((bh & 1) ? (tw & 0xffff0000u) : (tw << 16)) * LOG2E;
#pragma unroll 1
    for (int key = tid; key < SEQ; key += 512) {
        const u32x4 ka = *(const u32x4_a4*)(ksrc + key * 36), kb = *(const u32x4_a4*)(ksrc + key * 36 + 16);
        const unsigned kc = *(const unsigned*)(ksrc + key * 36 + 32);
        const float r = __builtin_amdgcn_rcpf(__builtin_amdgcn_sqrtf(sumsq9(ka, kb, kc)) + EPS);
        u32x4 a, b;
#pragma unroll
        for (int j = 0; j < 4; ++j) { a[j] = scale2(ka[j], r); b[j] = scale2(kb[j], r); }
        LDS_AT(u32x4, LDS_KM + (key >> 5) * 1024 + (key & 31) * 16) = a;
        LDS_AT(u32x4, LDS_KM + (key >> 5) * 1024 + (key & 31) * 16 + 512) = b;
        LDS_AT(unsigned, LDS_KE + key * 4) = scale2(kc, r);
        const int wv = LDS_VT + (key >> 3) * VBLK + (key & 7) * 2;
#pragma unroll 1
        for (int d = 0; d < DH; ++d) LDS_AT(uint16_t, wv + d * 16) = *(const uint16_t*)(vsrc + key * 36 + d * 2);
        LDS_AT(uint16_t, wv + 18 * 16) = 0x3F80;
    }
    u32x4 qma, qmb; u32x2 qe, ke;
    {
        const u32x4 qa = *(const u32x4_a4*)(qsrc + tid * 36), qb = *(const u32x4_a4*)(qsrc + tid * 36 + 16);
        const unsigned qcw = *(const unsigned*)(qsrc + tid * 36 + 32);
        const float rq = cs * __builtin_amdgcn_rcpf(__builtin_amdgcn_sqrtf(sumsq9(qa, qb, qcw)) + EPS);
#pragma unroll
        for (int j = 0; j < 4; ++j) {
            auto sw = __builtin_amdgcn_permlane32_swap(scale2(qa[j], rq), scale2(qb[j], rq), false, false);
            qma[j] = sw[0]; qmb[j] = sw[1];
        }
        qe[0] = scale2(qcw, rq); qe[1] = 0u; ke[1] = 0u;
    }
    __syncthreads();
    f32x16 oa = {}, ob = {};
    float ma = -INFINITY, mb = -INFINITY;
#pragma unroll 1
    for (int t = 0; t < NTILE; ++t) {
        ke[0] = LDS_AT(unsigned, LDS_KE + t * 128 + r32 * 4);
        const f32x32 s = MFMA4(ke, qe, (f32x32){});
        const u32x4 k0 = LDS_AT(u32x4, LDS_KM + t * 1024 + lane * 16);
        f32x16 sa = MFMA16(k0, qma, LO16(s)), sb = MFMA16(k0, qmb, HI16(s));
        float xa = sa[0], xb = sb[0];
#pragma unroll
        for (int r = 1; r < 16; ++r) { xa = __builtin_fmaxf(xa, sa[r]); xb = __builtin_fmaxf(xb, sb[r]); }
        xa = __builtin_fmaxf(xa, __shfl_xor(xa, 32)); xb = __builtin_fmaxf(xb, __shfl_xor(xb, 32));
        const float na = __builtin_fmaxf(ma, xa), nb = __builtin_fmaxf(mb, xb);
        const float fa = __builtin_amdgcn_exp2f(ma - na), fb = __builtin_amdgcn_exp2f(mb - nb);
        ma = na; mb = nb;
#pragma unroll
        for (int r = 0; r < 16; ++r) {
            oa[r] *= fa; sa[r] = __builtin_amdgcn_exp2f(sa[r] - na);
            ob[r] *= fb; sb[r] = __builtin_amdgcn_exp2f(sb[r] - nb);
        }
        const u32x4 w0 = LDS_AT(u32x4, LDS_VT + (t * 4 + hi) * VBLK + r32 * 16), w1 = LDS_AT(u32x4, LDS_VT + (t * 4 + 2 + hi) * VBLK + r32 * 16);
        oa = SMF(w0, LO8(sa), oa); ob = SMF(w0, LO8(sb), ob);
        oa = SMF(w1, HI8(sa), oa); ob = SMF(w1, HI8(sb), ob);
    }
    {
        float x[11], y[8];
#pragma unroll
        for (int r = 0; r < 11; ++r) {
            auto sw = __builtin_amdgcn_permlane32_swap(__float_as_uint(oa[r]), __float_as_uint(ob[r]), false, false);
            x[r] = __uint_as_float(sw[0]); if (r < 8) y[r] = __uint_as_float(sw[1]);
        }
        const float inv = __builtin_amdgcn_rcpf(x[10]);
        u32x4 a, b;
        a[0] = cvtpk(x[0] * inv, x[1] * inv); a[1] = cvtpk(x[2] * inv, x[3] * inv);
        a[2] = cvtpk(y[0] * inv, y[1] * inv); a[3] = cvtpk(y[2] * inv, y[3] * inv);
        b[0] = cvtpk(x[4] * inv, x[5] * inv); b[1] = cvtpk(x[6] * inv, x[7] * inv);
        b[2] = cvtpk(y[4] * inv, y[5] * inv); b[3] = cvtpk(y[6] * inv, y[7] * inv);
        char* op = odst + tid * 36;
        *(u32x4_a4*)(op) = a;
        *(u32x4_a4*)(op + 16) = b;
        *(unsigned*)(op + 32) = cvtpk(x[8] * inv, x[9] * inv);
    }
}

extern "C" void kernel_launch(void* const* d_in, const int* in_sizes, int n_in,
                              void* d_out, int out_size, void* d_ws, size_t ws_size,
                              hipStream_t stream) {
    (void)in_sizes; (void)n_in; (void)out_size; (void)d_ws; (void)ws_size;
    (void)hipFuncSetAttribute((const void*)qknorm_attn, hipFuncAttributeMaxDynamicSharedMemorySize, LDS_BYTES);
    qknorm_attn<<<NBH * 4, 512, LDS_BYTES, stream>>>((const uint16_t*)d_in[0], (const uint16_t*)d_in[1],
                                                     (const uint16_t*)d_in[2], (const uint16_t*)d_in[3],
                                                     (uint16_t*)d_out
                                                     );
}
```
